```python
import torch

def _softmax(x, axis=-1):
    return torch.softmax(x, dim=axis)

def _gelu(x):
    return torch.nn.functional.gelu(x, approximate='tanh')

def _silu(x):
    return torch.nn.functional.silu(x)

def _sigmoid(x):
    return torch.sigmoid(x)

def _layernorm(x, axis=-1, eps=1e-05):
    m = x.mean(dim=axis, keepdim=True)
    v = x.var(dim=axis, keepdim=True, unbiased=False)
    return (x - m) / torch.sqrt(v + eps)

def _scatter_add(tgt, idx, src, axis=0):
    return tgt.clone().index_add_(axis, idx, src)

def _segment_sum(src, seg, n):
    out = torch.zeros((n,) + src.shape[1:], dtype=src.dtype)
    return out.index_add_(0, seg, src)

def _pad(x, pw):
    flat = tuple((v for pair in reversed(pw) for v in pair))
    return torch.nn.functional.pad(x, flat)
kernel_name = 'adaLN_cond__spectral_gfnet__moe_arch_mod__prompt'

def setup_inputs():

    _w = lambda a: (lambda t: t.to(torch.bfloat16) if t.is_floating_point() else t)(torch.as_tensor(a))

    def _A_setup():
        B, S, D = (1, 2048, 640)
        Df = D // 2 + 1
        return {'x': _w((lambda *s: torch.randn(*s, dtype=torch.bfloat16) * 0.05)(B, S, D)), 'w_r': _w((lambda *s: torch.randn(*s, dtype=torch.bfloat16) * 3.2)(S, Df) * 0.02), 'w_i': _w((lambda *s: torch.randn(*s, dtype=torch.bfloat16) * 3.2)(S, Df) * 0.02)}
    _A_in = _A_setup()

    def _B_setup():
        B, S, D, F, C = (1, 2048, 640, 1536, 1)
        return {'x': _w((lambda *s: torch.randn(*s, dtype=torch.bfloat16) * 0.05)(B, S, D)), 'w_r': _w((lambda *s: torch.randn(*s, dtype=torch.bfloat16) * 0.05)(D)), 'w_gate': _w((lambda *s: torch.randn(*s, dtype=torch.bfloat16) * 0.05)(D, F)), 'w_up': _w((lambda *s: torch.randn(*s, dtype=torch.bfloat16) * 0.05)(D, F)), 'w_down': _w((lambda *s: torch.randn(*s, dtype=torch.bfloat16) * 0.05)(F, D))}
    _B_in = _B_setup()
    return {'A_x': _A_in['x'], 'A_w_r': _A_in['w_r'], 'A_w_i': _A_in['w_i'], 'B_x': _B_in['x'], 'B_w_r': _B_in['w_r'], 'B_w_gate': _B_in['w_gate'], 'B_w_up': _B_in['w_up'], 'B_w_down': _B_in['w_down'], 'c': _w((lambda *s: torch.randn(*s, dtype=torch.bfloat16) * 0.1)(1, 640) * 8.0), 'w_mod': _w((lambda *s: torch.randn(*s, dtype=torch.bfloat16) * 0.1)(640, 6 * 640) * 640 ** (-0.5) * 8.0)}

def reference(A_x, A_w_r, A_w_i, B_x, B_w_r, B_w_gate, B_w_up, B_w_down, c, w_mod):

    def _A_ref(x, w_r, w_i):
        B, S, D = x.shape
        X = (lambda x, axes=(-2, -1): torch.fft.rfft2(x.float(), dim=axes))(x, axes=(-2, -1))
        W = w_r + 1j * w_i
        X = X * W
        y = (lambda x, s, axes=(-2, -1): torch.fft.irfft2(x, s=s, dim=axes).to(torch.bfloat16))(X, (S, D), axes=(-2, -1))
        return y

    def _B_ref(x, w_r, w_gate, w_up, w_down):
        B, S, D = x.shape
        C = 1
        r = x @ w_r
        idx = torch.argsort(-r, axis=-1)[:, :C]
        idx_e = (lambda x, n, axis: torch.repeat_interleave(x, n, dim=axis))(idx.reshape(B, C, 1), D, axis=2)
        x_sel = (lambda x, i, axis: torch.take_along_dim(x, i, dim=axis))(x, idx_e, axis=1)
        g = _silu(x_sel @ w_gate)
        u = x_sel @ w_up
        y = g * u @ w_down
        r_sel = _sigmoid((lambda x, i, axis: torch.take_along_dim(x, i, dim=axis))(r, idx, axis=1))
        y = y * r_sel.reshape(B, C, 1)
        b_off = (torch.arange(B) * S).reshape(B, 1)
        flat_idx = (idx + b_off).reshape(B * C)
        zero = (x * 0.0).reshape(B * S, D)
        upd = _scatter_add(zero, flat_idx, y.reshape(B * C, D), axis=0)
        return x + upd.reshape(B, S, D)
    x = A_x
    mod = (_silu(c) @ w_mod).reshape(c.shape[0], 6, 1, -1)
    sa, ca, ga, sb, cb, gb = (mod[:, 0].clone(), mod[:, 1].clone(), mod[:, 2].clone(), mod[:, 3].clone(), mod[:, 4].clone(), mod[:, 5].clone())
    h = _layernorm(x, axis=-1) * (1 + ca) + sa
    x = x + ga * _A_ref(h, A_w_r, A_w_i)
    h = _layernorm(x, axis=-1) * (1 + cb) + sb
    x = x + gb * _B_ref(h, B_w_r, B_w_gate, B_w_up, B_w_down)
    return x
```

```cpp
#include <hip/hip_runtime.h>
#include <stdint.h>

typedef unsigned u32;
typedef unsigned short u16;
typedef u32 u32x2 __attribute__((ext_vector_type(2)));
typedef u32 u32x4 __attribute__((ext_vector_type(4)));
typedef float f32x2 __attribute__((ext_vector_type(2)));
typedef float f32x4 __attribute__((ext_vector_type(4)));
typedef float f32x16 __attribute__((ext_vector_type(16)));
typedef __bf16 bf16x2 __attribute__((ext_vector_type(2)));
typedef __bf16 bf16x8 __attribute__((ext_vector_type(8)));
#define DI __device__ __forceinline__

constexpr int S = 2048, D = 640, DF = 321, F = 1536;
constexpr int NSEG = 168;
constexpr float EPS = 1e-5f;

DI u32 pk(float lo, float hi) { return __builtin_bit_cast(u32, __builtin_convertvector((f32x2){lo, hi}, bf16x2)); }
DI float lo16(u32 v) { return __builtin_bit_cast(float, v << 16); }
DI float hi16(u32 v) { return __builtin_bit_cast(float, v & 0xffff0000u); }
DI float bfr(float x) { return lo16(pk(x, 0.f)); }
DI float ldbf(const u16* p) { return lo16(*p); }
DI f32x16 mfma32(u32x4 a, u32x4 b, f32x16 c) {
  return __builtin_amdgcn_mfma_f32_32x32x16_bf16(__builtin_bit_cast(bf16x8, a), __builtin_bit_cast(bf16x8, b), c, 0, 0, 0);
}
DI f32x4 mfma16(u32x4 a, u32x4 b, f32x4 c) {
  return __builtin_amdgcn_mfma_f32_16x16x32_bf16(__builtin_bit_cast(bf16x8, a), __builtin_bit_cast(bf16x8, b), c, 0, 0, 0);
}
DI float dot2(u32 a, u32 b, float c) {
  return __builtin_amdgcn_fdot2_f32_bf16(__builtin_bit_cast(bf16x2, a), __builtin_bit_cast(bf16x2, b), c, false);
}
DI float cosr(float rev) { return __builtin_amdgcn_cosf(rev); }
DI float sinr(float rev) { return __builtin_amdgcn_sinf(rev); }
template <int CTRL, int RM> DI float dpp_add(float v) {
  return v + __builtin_bit_cast(float, __builtin_amdgcn_update_dpp(0, __builtin_bit_cast(int, v), CTRL, RM, 0xf, true));
}
DI float wave_sum(float v) {
  v = dpp_add<0xB1, 0xf>(v);
  v = dpp_add<0x4E, 0xf>(v);
  v = dpp_add<0x141, 0xf>(v);
  v = dpp_add<0x140, 0xf>(v);
  v = dpp_add<0x142, 0xa>(v);
  v = dpp_add<0x143, 0xc>(v);
  return __builtin_bit_cast(float, __builtin_amdgcn_readlane(__builtin_bit_cast(int, v), 63));
}
DI float silu(float x) { return x / (1.f + __expf(-x)); }
DI int xcd_map(int b, int per) { return (b & 7) * per + (b >> 3); }

DI void col_info(int g, int c, int& k1, int& kap, bool& ok) {
  if (g == 0) { k1 = c >= 12 ? 4 : 0; kap = c >= 12 ? c - 12 : c; ok = kap < 11; }
  else { k1 = g; kap = c; ok = c < 20; }
}
DI int seg_index(int g, int t, int h, int c) {
  const int n = g == 0 ? 6 : 5, base = g == 0 ? 0 : 48 + 40 * (g - 1);
  return base + (2 * t + h) * n + (c >> 2);
}

struct Ws {
  float* mod;
  u32* T1;
  u32* T2;
  float* cand_r;
  int* cand_i;
  float* h2;
  float* ob;
  float* act;
};

__global__ __launch_bounds__(256) void k_mod(const u16* __restrict__ c, const u16* __restrict__ wmod, float* __restrict__ mod) {
  __shared__ float red[4][16];
  const int b = xcd_map(blockIdx.x, 30), t = threadIdx.x, half = t & 1, r0 = t >> 1;
  float acc[8] = {0, 0, 0, 0, 0, 0, 0, 0};
  u32x4 w[5];
  float sc[5];
#pragma unroll
  for (int i = 0; i < 5; ++i) {
    const int k = r0 + 128 * i;
    w[i] = *(const u32x4*)(wmod + (size_t)k * 3840 + b * 16 + half * 8);
    sc[i] = bfr(silu(ldbf(c + k)));
  }
#pragma unroll
  for (int i = 0; i < 5; ++i)
#pragma unroll
    for (int j = 0; j < 4; ++j) { acc[2 * j] += sc[i] * lo16(w[i][j]); acc[2 * j + 1] += sc[i] * hi16(w[i][j]); }
#pragma unroll
  for (int j = 0; j < 8; ++j)
    for (int o = 2; o < 64; o <<= 1) acc[j] += __shfl_xor(acc[j], o);
  if ((t & 63) < 2)
#pragma unroll
    for (int j = 0; j < 8; ++j) red[t >> 6][half * 8 + j] = acc[j];
  __syncthreads();
  if (t < 16) mod[b * 16 + t] = bfr(red[0][t] + red[1][t] + red[2][t] + red[3][t]);
}

__global__ __launch_bounds__(512) void k_p1(const u16* __restrict__ x, const float* __restrict__ mod, u32* __restrict__ T1) {
  __shared__ __attribute__((aligned(16))) float Z[8 * 640];
  __shared__ __attribute__((aligned(16))) u32 U[4 * 640];
  const int s2 = xcd_map(blockIdx.x, 32), t = threadIdx.x, l = t & 63;
  const int w = __builtin_amdgcn_readfirstlane(t >> 6);
  {
    const u16* row = x + (size_t)(s2 + 256 * w) * D;
    const u32x2 a = *(const u32x2*)(row + 4 * l), b = *(const u32x2*)(row + 256 + 4 * l);
    const u32 c = *(const u32*)(row + 512 + 2 * l);
    float v[10] = {lo16(a[0]), hi16(a[0]), lo16(a[1]), hi16(a[1]), lo16(b[0]), hi16(b[0]), lo16(b[1]), hi16(b[1]), lo16(c), hi16(c)};
    float s = 0, q = 0;
#pragma unroll
    for (int i = 0; i < 10; ++i) { s += v[i]; q += v[i] * v[i]; }
    s = wave_sum(s); q = wave_sum(q);
    const float m = s * (1.f / D), rs = __builtin_amdgcn_rsqf(fmaxf(q * (1.f / D) - m * m, 0.f) + EPS);
#pragma unroll
    for (int i = 0; i < 10; ++i) v[i] = (v[i] - m) * rs;
    *(f32x4*)(Z + w * 640 + 4 * l) = (f32x4){v[0], v[1], v[2], v[3]};
    *(f32x4*)(Z + w * 640 + 256 + 4 * l) = (f32x4){v[4], v[5], v[6], v[7]};
    *(f32x2*)(Z + w * 640 + 512 + 2 * l) = (f32x2){v[8], v[9]};
  }
  __syncthreads();
  if (w < 5) {
    f32x2 z[8];
#pragma unroll
    for (int i = 0; i < 8; ++i) z[i] = *(const f32x2*)(Z + i * 640 + 2 * t);
    const f32x2 sa = *(const f32x2*)(mod + 2 * t), ca = *(const f32x2*)(mod + 640 + 2 * t);
    const f32x2 A = {bfr(1.f + ca[0]), bfr(1.f + ca[1])};
    const f32x2 a0 = z[0] + z[4], a1 = z[0] - z[4], b0 = z[2] + z[6], b1 = z[2] - z[6];
    const f32x2 c0 = z[1] + z[5], c1 = z[1] - z[5], d0 = z[3] + z[7], d1 = z[3] - z[7];
    const f32x2 e = a0 + b0, f = c0 + d0, p = (c1 - d1) * 0.70710678f, q = (c1 + d1) * 0.70710678f;
    const f32x2 re[4] = {(e + f) * A + 8.f * sa, (a1 + p) * A, (a0 - b0) * A, (a1 - p) * A};
    const f32x2 im[4] = {(e - f) * A, (-b1 - q) * A, (d0 - c0) * A, (b1 - q) * A};
    const int off = (t & 15) * 20 + (t >> 4);
#pragma unroll
    for (int g = 0; g < 4; ++g) {
      U[g * 640 + off] = pk(re[g][0], im[g][0]);
      U[g * 640 + 320 + off] = pk(re[g][1], im[g][1]);
    }
  }
  __syncthreads();
  if (w >= 4) return;
  const int g = w, c = l & 31, h = l >> 5;
  int k1, kap; bool ok;
  col_info(g, c, k1, kap, ok);
  f32x16 yr = {}, yi = {};
#pragma unroll
  for (int tau = 0; tau < 3; ++tau) {
    const u32x4 a = *(const u32x4*)(U + g * 640 + c * 20 + (tau == 2 ? 16 : 8 * tau + 4 * h));
    u32x4 br, bi;
#pragma unroll
    for (int j = 0; j < 4; ++j) {
      const int n1 = 8 * tau + 4 * h + j;
      const float th = (float)((n1 * kap) % 20) * 0.05f;
      const bool v = ok && n1 < 20;
      const float cs = v ? cosr(th) : 0.f, sn = v ? sinr(th) : 0.f;
      if (g == 0) { br[j] = k1 ? pk(0.f, cs) : pk(cs, 0.f); bi[j] = k1 ? pk(0.f, -sn) : pk(-sn, 0.f); }
      else { br[j] = pk(cs, sn); bi[j] = pk(-sn, cs); }
    }
    yr = mfma32(a, br, yr);
    yi = mfma32(a, bi, yi);
  }
  f32x16 zr = {}, zi = {};
#pragma unroll
  for (int tau = 0; tau < 4; ++tau) {
    u32x4 b, ar, ai;
#pragma unroll
    for (int j = 0; j < 4; ++j) {
      const int rho = 8 * tau + 4 * h + j, n2 = 2 * (rho & 15) + (rho >> 4);
      const float th = (float)((16 * n2 * kap + 5 * s2 * k1) % 10240) * (1.f / 10240.f);
      const float cs = cosr(th), sn = sinr(th);
      const float r = yr[4 * tau + j], i = yi[4 * tau + j];
      b[j] = pk(r * cs + i * sn, i * cs - r * sn);
      const float t2 = (float)((n2 * c) & 31) * (1.f / 32.f);
      const float c2 = cosr(t2), s2_ = sinr(t2);
      ar[j] = pk(c2, s2_); ai[j] = pk(-s2_, c2);
    }
    zr = mfma32(ar, b, zr);
    zi = mfma32(ai, b, zi);
  }
  if (c < (g == 0 ? 24 : 20)) {
#pragma unroll
    for (int tt = 0; tt < 4; ++tt) {
      u32x4 o;
#pragma unroll
      for (int j = 0; j < 4; ++j) o[j] = pk(zr[4 * tt + j], zi[4 * tt + j]);
      *(u32x4*)(T1 + ((size_t)s2 * NSEG + seg_index(g, tt, h, c)) * 16 + (c & 3) * 4) = o;
    }
  }
}

__global__ __launch_bounds__(512) void k_p2(const u32* __restrict__ T1, const u16* __restrict__ wr, const u16* __restrict__ wi,
                                           u32* __restrict__ T2) {
  __shared__ __attribute__((aligned(16))) u32 L[256 * 16];
  const int seg = xcd_map(blockIdx.x, 21), t = threadIdx.x, l = t & 63;
  const int w = __builtin_amdgcn_readfirstlane(t >> 6);
  int g, th, c0;
  if (seg < 48) { g = 0; th = seg / 6; c0 = (seg % 6) * 4; }
  else { g = 1 + (seg - 48) / 40; th = ((seg - 48) % 40) / 5; c0 = ((seg - 48) % 5) * 4; }
  const int k1 = g ? g : (c0 >= 12 ? 4 : 0), kap0 = (g == 0 && c0 >= 12) ? c0 - 12 : c0, lam0 = 4 * th;
#pragma unroll
  for (int i = 0; i < 2; ++i) {
    const int r = (t >> 2) + 128 * i, c = t & 3;
    const u32x4 v = *(const u32x4*)(T1 + ((size_t)r * NSEG + seg) * 16 + c * 4);
    *(u32x4*)(L + r * 16 + ((c ^ ((r >> 2) & 3)) << 2)) = v;
  }
  const int m = l & 15, gam = l >> 4, c = w >> 1, lp = (w & 1) * 2;
  float fr[2][4], fi[2][4];
#pragma unroll
  for (int u = 0; u < 2; ++u) {
    const int kd = kap0 + c + 20 * (lam0 + lp + u);
#pragma unroll
    for (int r = 0; r < 4; ++r) {
      const int ks = k1 + 8 * m + 128 * (4 * gam + r), kn = (S - ks) & (S - 1);
      if (kd == 0 || kd == 320) {
        fr[u][r] = 0.5f * (ldbf(wr + ks * DF + kd) + ldbf(wr + kn * DF + kd));
        fi[u][r] = 0.5f * (ldbf(wi + ks * DF + kd) - ldbf(wi + kn * DF + kd));
      } else if (kd < 320) {
        fr[u][r] = ldbf(wr + ks * DF + kd); fi[u][r] = ldbf(wi + ks * DF + kd);
      } else {
        fr[u][r] = ldbf(wr + kn * DF + D - kd); fi[u][r] = -ldbf(wi + kn * DF + D - kd);
      }
    }
  }
  u32x4 Fr, Fi, Gr, Gi;
  float tc[4], ts[4];
#pragma unroll
  for (int j = 0; j < 4; ++j) {
    const int e = m * (4 * gam + j);
    const float a = (float)(e & 15) * (1.f / 16.f), cs = cosr(a), sn = sinr(a);
    Fr[j] = pk(cs, sn); Fi[j] = pk(-sn, cs); Gr[j] = pk(cs, -sn); Gi[j] = pk(sn, cs);
    const float b = (float)e * (1.f / 256.f);
    tc[j] = cosr(b); ts[j] = sinr(b);
  }
  __syncthreads();
  u32x2 in[4];
  const int lo = m * 16 + ((c ^ (m >> 2)) << 2) + lp;
#pragma unroll
  for (int j = 0; j < 4; ++j) in[j] = *(const u32x2*)(L + (4 * gam + j) * 256 + lo);
  u32x2 out[4];
#pragma unroll
  for (int u = 0; u < 2; ++u) {
    const f32x4 z = {0, 0, 0, 0};
    u32x4 a = {in[0][u], in[1][u], in[2][u], in[3][u]};
    f32x4 r = mfma16(a, Fr, z), i = mfma16(a, Fi, z);
#pragma unroll
    for (int j = 0; j < 4; ++j) a[j] = pk(r[j] * tc[j] + i[j] * ts[j], i[j] * tc[j] - r[j] * ts[j]);
    r = mfma16(Fr, a, z); i = mfma16(Fi, a, z);
#pragma unroll
    for (int j = 0; j < 4; ++j) a[j] = pk(r[j] * fr[u][j] - i[j] * fi[u][j], r[j] * fi[u][j] + i[j] * fr[u][j]);
    r = mfma16(a, Gr, z); i = mfma16(a, Gi, z);
#pragma unroll
    for (int j = 0; j < 4; ++j) a[j] = pk(r[j] * tc[j] - i[j] * ts[j], i[j] * tc[j] + r[j] * ts[j]);
    r = mfma16(Gr, a, z); i = mfma16(Gi, a, z);
#pragma unroll
    for (int j = 0; j < 4; ++j) out[j][u] = pk(r[j], i[j]);
  }
#pragma unroll
  for (int j = 0; j < 4; ++j) *(u32x2*)(L + (4 * gam + j) * 256 + lo) = out[j];
  __syncthreads();
#pragma unroll
  for (int i = 0; i < 2; ++i) {
    const int r = (t >> 2) + 128 * i, cc = t & 3;
    *(u32x4*)(T2 + ((size_t)seg * 256 + r) * 16 + cc * 4) = *(const u32x4*)(L + r * 16 + ((cc ^ ((r >> 2) & 3)) << 2));
  }
}

DI void ld10f(const float* p, int l, float (&v)[10]) {
  const f32x4 a = *(const f32x4*)(p + 4 * l), b = *(const f32x4*)(p + 256 + 4 * l);
  const f32x2 c = *(const f32x2*)(p + 512 + 2 * l);
  v[0] = a[0]; v[1] = a[1]; v[2] = a[2]; v[3] = a[3]; v[4] = b[0]; v[5] = b[1]; v[6] = b[2]; v[7] = b[3]; v[8] = c[0]; v[9] = c[1];
}
DI void st10f(float* p, int l, const float (&v)[10]) {
  *(f32x4*)(p + 4 * l) = (f32x4){v[0], v[1], v[2], v[3]};
  *(f32x4*)(p + 256 + 4 * l) = (f32x4){v[4], v[5], v[6], v[7]};
  *(f32x2*)(p + 512 + 2 * l) = (f32x2){v[8], v[9]};
}
DI void ld10b(const u16* row, int l, float (&v)[10]) {
  const u32x2 a = *(const u32x2*)(row + 4 * l), b = *(const u32x2*)(row + 256 + 4 * l);
  const u32 c = *(const u32*)(row + 512 + 2 * l);
  v[0] = lo16(a[0]); v[1] = hi16(a[0]); v[2] = lo16(a[1]); v[3] = hi16(a[1]);
  v[4] = lo16(b[0]); v[5] = hi16(b[0]); v[6] = lo16(b[1]); v[7] = hi16(b[1]); v[8] = lo16(c); v[9] = hi16(c);
}

__global__ __launch_bounds__(512) void k_p3(const u16* __restrict__ x, const u16* __restrict__ wrt, Ws ws, u16* __restrict__ out) {
  __shared__ __attribute__((aligned(16))) u32 E[4 * 640];
  __shared__ float rr[8];
  const int s2 = xcd_map(blockIdx.x, 32), t = threadIdx.x, l = t & 63;
  const int w = __builtin_amdgcn_readfirstlane(t >> 6);
  float xv[10], ga[10], cb[10], sb[10], gb[10], vv[10];
  ld10b(x + (size_t)(s2 + 256 * w) * D, l, xv);
  ld10b(wrt, l, vv);
  ld10f(ws.mod + 2 * 640, l, ga); ld10f(ws.mod + 3 * 640, l, sb); ld10f(ws.mod + 4 * 640, l, cb); ld10f(ws.mod + 5 * 640, l, gb);
  if (w < 4) {
    const int g = w, c = l & 31, h = l >> 5;
    u32x4 a[4];
#pragma unroll
    for (int tt = 0; tt < 4; ++tt) {
      a[tt] = (u32x4){0, 0, 0, 0};
      if (c < (g == 0 ? 24 : 20)) a[tt] = *(const u32x4*)(ws.T2 + ((size_t)seg_index(g, tt, h, c) * 256 + s2) * 16 + (c & 3) * 4);
    }
    f32x16 yr = {}, yi = {};
#pragma unroll
    for (int tt = 0; tt < 4; ++tt) {
      u32x4 br, bi;
#pragma unroll
      for (int j = 0; j < 4; ++j) {
        const float th = (float)(((8 * tt + 4 * h + j) * c) & 31) * (1.f / 32.f), cs = cosr(th), sn = sinr(th);
        br[j] = pk(cs, -sn); bi[j] = pk(sn, cs);
      }
      yr = mfma32(a[tt], br, yr);
      yi = mfma32(a[tt], bi, yi);
    }
    f32x16 zr = {}, zi = {};
#pragma unroll
    for (int tau = 0; tau < 3; ++tau) {
      u32x4 b, ar, ai;
#pragma unroll
      for (int j = 0; j < 4; ++j) {
        int k1, kap; bool ok;
        col_info(g, 8 * tau + 4 * h + j, k1, kap, ok);
        const float th = (float)((16 * c * kap + 5 * s2 * k1) % 10240) * (1.f / 10240.f), cs = cosr(th), sn = sinr(th);
        const float r = yr[4 * tau + j], i = yi[4 * tau + j];
        b[j] = pk(r * cs - i * sn, i * cs + r * sn);
        const float t2 = (float)((c * kap) % 20) * 0.05f;
        const float c2 = ok ? cosr(t2) : 0.f, s2_ = ok ? sinr(t2) : 0.f;
        if (g == 0) {
          const float cf = (kap == 0 || kap == 10) ? 1.f : 2.f;
          const u32 p = pk(cf * c2, -cf * s2_);
          ar[j] = k1 ? 0u : p; ai[j] = k1 ? p : 0u;
        } else { ar[j] = pk(c2, -s2_); ai[j] = pk(s2_, c2); }
      }
      zr = mfma32(ar, b, zr);
      zi = mfma32(ai, b, zi);
    }
#pragma unroll
    for (int r = 0; r < 12; ++r) {
      const int n1 = 8 * (r >> 2) + 4 * h + (r & 3);
      if (n1 < 20) E[g * 640 + n1 * 32 + c] = pk(zr[r], zi[r]);
    }
  }
  float sv = 0, sbw = 0;
#pragma unroll
  for (int i = 0; i < 10; ++i) {
    cb[i] = bfr(1.f + cb[i]);
    sbw += sb[i] * vv[i];
    vv[i] *= cb[i];
    sv += vv[i];
    ga[i] *= 1.f / ((float)S * (float)D);
  }
  sv = wave_sum(sv); sbw = wave_sum(sbw);
  u32 cf[4];
  cf[0] = pk(1.f, (w & 1) ? -1.f : 1.f);
#pragma unroll
  for (int g = 1; g < 4; ++g) { const float th = (float)((w * g) & 7) * 0.125f; cf[g] = pk(2.f * cosr(th), -2.f * sinr(th)); }
  __syncthreads();
  float y[10] = {0, 0, 0, 0, 0, 0, 0, 0, 0, 0};
#pragma unroll
  for (int g = 0; g < 4; ++g) {
    const u32x4 a = *(const u32x4*)(E + g * 640 + 4 * l), b = *(const u32x4*)(E + g * 640 + 256 + 4 * l);
    const u32x2 c = *(const u32x2*)(E + g * 640 + 512 + 2 * l);
#pragma unroll
    for (int j = 0; j < 4; ++j) { y[j] = dot2(a[j], cf[g], y[j]); y[4 + j] = dot2(b[j], cf[g], y[4 + j]); }
    y[8] = dot2(c[0], cf[g], y[8]); y[9] = dot2(c[1], cf[g], y[9]);
  }
  float s = 0, q = 0, dt = 0;
#pragma unroll
  for (int i = 0; i < 10; ++i) { xv[i] += ga[i] * y[i]; s += xv[i]; q += xv[i] * xv[i]; dt += xv[i] * vv[i]; }
  s = wave_sum(s); q = wave_sum(q); dt = wave_sum(dt);
  const float m = s * (1.f / D), rs = __builtin_amdgcn_rsqf(fmaxf(q * (1.f / D) - m * m, 0.f) + EPS);
  const float r = rs * (dt - m * sv) + sbw;
  float h2[10], o[10];
#pragma unroll
  for (int i = 0; i < 10; ++i) { h2[i] = (xv[i] - m) * rs * cb[i] + sb[i]; o[i] = xv[i] + gb[i] * h2[i]; }
  u16* orow = out + (size_t)(s2 + 256 * w) * D;
  *(u32x2*)(orow + 4 * l) = (u32x2){pk(o[0], o[1]), pk(o[2], o[3])};
  *(u32x2*)(orow + 256 + 4 * l) = (u32x2){pk(o[4], o[5]), pk(o[6], o[7])};
  *(u32*)(orow + 512 + 2 * l) = pk(o[8], o[9]);
  if (l == 0) rr[w] = r;
  __syncthreads();
  int best = 0; float br = rr[0];
#pragma unroll
  for (int i = 1; i < 8; ++i) if (rr[i] > br) { br = rr[i]; best = i; }
  if (best == w) {
    st10f(ws.h2 + s2 * 640, l, h2);
    st10f(ws.ob + s2 * 640, l, o);
    if (l == 0) { ws.cand_r[s2] = r; ws.cand_i[s2] = s2 + 256 * w; }
  }
}

DI int block_argmax(const float* cand_r, float* sr, int* si, float& rbest) {
  const int t = threadIdx.x;
  if (t < 64) {
    float b = cand_r[t]; int bi = t;
    for (int i = 1; i < 4; ++i) { const float v = cand_r[t + 64 * i]; if (v > b) { b = v; bi = t + 64 * i; } }
    for (int o = 1; o < 64; o <<= 1) {
      const float v = __shfl_xor(b, o); const int vi = __shfl_xor(bi, o);
      if (v > b || (v == b && vi < bi)) { b = v; bi = vi; }
    }
    if (t == 0) { *sr = b; *si = bi; }
  }
  __syncthreads();
  rbest = *sr;
  return *si;
}

__global__ __launch_bounds__(320) void k_gu(const u16* __restrict__ wg, const u16* __restrict__ wu, Ws ws) {
  __shared__ float sr; __shared__ int si; __shared__ float red[5][16];
  const int b = xcd_map(blockIdx.x, 24), t = threadIdx.x;
  u32x4 w[4];
#pragma unroll
  for (int i = 0; i < 2; ++i) {
    w[i] = *(const u32x4*)(wg + (size_t)(t + 320 * i) * F + b * 8);
    w[2 + i] = *(const u32x4*)(wu + (size_t)(t + 320 * i) * F + b * 8);
  }
  float rb;
  const int blk = block_argmax(ws.cand_r, &sr, &si, rb);
  const float h0 = bfr(ws.h2[blk * 640 + t]), h1 = bfr(ws.h2[blk * 640 + t + 320]);
#pragma unroll
  for (int mtx = 0; mtx < 2; ++mtx)
#pragma unroll
    for (int j = 0; j < 4; ++j) {
      const float a = wave_sum(h0 * lo16(w[2 * mtx][j]) + h1 * lo16(w[2 * mtx + 1][j]));
      const float c = wave_sum(h0 * hi16(w[2 * mtx][j]) + h1 * hi16(w[2 * mtx + 1][j]));
      if ((t & 63) == 0) { red[t >> 6][mtx * 8 + 2 * j] = a; red[t >> 6][mtx * 8 + 2 * j + 1] = c; }
    }
  __syncthreads();
  if (t < 8) {
    float g = 0, u = 0;
    for (int i = 0; i < 5; ++i) { g += red[i][t]; u += red[i][8 + t]; }
    ws.act[b * 8 + t] = bfr(bfr(silu(bfr(g))) * bfr(u));
  }
}

__global__ __launch_bounds__(512) void k_dn(const u16* __restrict__ wd, Ws ws, u16* __restrict__ out) {
  __shared__ float sr; __shared__ int si; __shared__ float red[8][16];
  const int b = xcd_map(blockIdx.x, 5), t = threadIdx.x, half = t & 1, r0 = t >> 1;
  u32x4 w[6];
  float a[6];
#pragma unroll
  for (int i = 0; i < 6; ++i) { w[i] = *(const u32x4*)(wd + (size_t)(r0 + 256 * i) * D + b * 16 + half * 8); a[i] = ws.act[r0 + 256 * i]; }
  float rb;
  const int blk = block_argmax(ws.cand_r, &sr, &si, rb);
  float acc[8] = {0, 0, 0, 0, 0, 0, 0, 0};
#pragma unroll
  for (int i = 0; i < 6; ++i)
#pragma unroll
    for (int j = 0; j < 4; ++j) { acc[2 * j] += a[i] * lo16(w[i][j]); acc[2 * j + 1] += a[i] * hi16(w[i][j]); }
#pragma unroll
  for (int j = 0; j < 8; ++j)
    for (int o = 2; o < 64; o <<= 1) acc[j] += __shfl_xor(acc[j], o);
  if ((t & 63) < 2)
#pragma unroll
    for (int j = 0; j < 8; ++j) red[t >> 6][half * 8 + j] = acc[j];
  __syncthreads();
  if (t < 16) {
    float y = 0;
    for (int i = 0; i < 8; ++i) y += red[i][t];
    const int d = b * 16 + t;
    const float sg = 1.f / (1.f + __expf(-bfr(rb)));
    const float o = ws.ob[blk * 640 + d] + ws.mod[5 * 640 + d] * (y * sg);
    out[(size_t)ws.cand_i[blk] * D + d] = (u16)pk(o, 0.f);
  }
}

extern "C" void kernel_launch(void* const* d_in, const int* in_sizes, int n_in, void* d_out, int out_size, void* d_ws, size_t ws_size,
                              hipStream_t stream) {
  const u16 *x = (const u16*)d_in[0], *wr = (const u16*)d_in[1], *wi = (const u16*)d_in[2];
  const u16 *wrt = (const u16*)d_in[4], *wg = (const u16*)d_in[5], *wu = (const u16*)d_in[6], *wd = (const u16*)d_in[7];
  const u16 *c = (const u16*)d_in[8], *wmod = (const u16*)d_in[9];
  char* p = (char*)d_ws;
  Ws ws;
  ws.mod = (float*)p;
  ws.cand_r = (float*)(p + 16384);
  ws.cand_i = (int*)(p + 16384 + 1024);
  ws.act = (float*)(p + 20480);
  ws.h2 = (float*)(p + 32768);
  ws.ob = (float*)(p + 32768 + 655360);
  ws.T1 = (u32*)(p + (2 << 20));
  ws.T2 = (u32*)(p + (6 << 20));
  k_mod<<<240, 256, 0, stream>>>(c, wmod, ws.mod);
  k_p1<<<256, 512, 0, stream>>>(x, ws.mod, ws.T1);
  k_p2<<<NSEG, 512, 0, stream>>>(ws.T1, wr, wi, ws.T2);
  k_p3<<<256, 512, 0, stream>>>(x, wrt, ws, (u16*)d_out);
  k_gu<<<192, 320, 0, stream>>>(wg, wu, ws);
  k_dn<<<40, 512, 0, stream>>>(wd, ws, (u16*)d_out);
}
```

```cpp
#include <hip/hip_runtime.h>
#include <stdint.h>

typedef unsigned u32;
typedef unsigned short u16;
typedef unsigned long long u64;
typedef u32 u32x2 __attribute__((ext_vector_type(2)));
typedef u32 u32x4 __attribute__((ext_vector_type(4)));
typedef u32 u32x2u __attribute__((ext_vector_type(2), aligned(2)));
typedef float f32x2 __attribute__((ext_vector_type(2)));
typedef float f32x4 __attribute__((ext_vector_type(4)));
typedef float f32x16 __attribute__((ext_vector_type(16)));
typedef short s16x4 __attribute__((ext_vector_type(4)));
typedef __bf16 bf16x2 __attribute__((ext_vector_type(2)));
typedef __bf16 bf16x8 __attribute__((ext_vector_type(8)));
typedef __amdgpu_buffer_rsrc_t rsrc_t;
#define DI __device__ __forceinline__
#define LAS __attribute__((address_space(3)))

constexpr int S = 2048, D = 640, DF = 321, F = 1536;
constexpr float EPS = 1e-5f;
constexpr u32 SENT = 0xFFFFFFFFu, NONE = 0xFFFFFF00u;
constexpr int SPIN = 1 << 16;

constexpr u32 O_EPOCH = 0;
constexpr u32 O_TOK = 4096;
constexpr u32 O_DONE = 8192;
constexpr u32 O_CAND = 16384, CAND_REP = 4096;
constexpr u32 O_ACT = 49152, ACT_REP = 4096;
constexpr u32 O_MOD = 81920, MOD_REP = 16384;
constexpr u32 O_H2 = 262144;
constexpr u32 SEGB = 16384 + 256;
constexpr u32 O_T1 = 1u << 20, O_T2 = 4u << 20;
constexpr u32 O_STAMP = 7u << 20;
constexpr u32 WS_USED = 8u << 20;

constexpr int L_Z = 0;
constexpr int L_U = 5120;
constexpr int L_X = 7680;
constexpr int L_W = 11776;
constexpr int L_M = 15872;
constexpr int L_R = 17152;
constexpr int L_Q = 17664;
constexpr int L_T = 17920;
constexpr int L_END = 30208;

DI u32 pk(float lo, float hi) { return __builtin_bit_cast(u32, __builtin_convertvector((f32x2){lo, hi}, bf16x2)); }
DI float asf(u32 v) { return __builtin_bit_cast(float, v); }
DI u32 asu(float v) { return __builtin_bit_cast(u32, v); }
DI float lo16(u32 v) { return asf(v << 16); }
DI float hi16(u32 v) { return asf(v & 0xffff0000u); }
DI float bfr(float x) { return lo16(pk(x, 0.f)); }
DI float ldbf(const u16* p) { return lo16(*p); }
DI f32x16 mfma32(u32x4 a, u32x4 b, f32x16 c) {
  return __builtin_amdgcn_mfma_f32_32x32x16_bf16(__builtin_bit_cast(bf16x8, a), __builtin_bit_cast(bf16x8, b), c, 0, 0, 0);
}
DI f32x4 mfma16(u32x4 a, u32x4 b, f32x4 c) {
  return __builtin_amdgcn_mfma_f32_16x16x32_bf16(__builtin_bit_cast(bf16x8, a), __builtin_bit_cast(bf16x8, b), c, 0, 0, 0);
}
DI float dot2(u32 a, u32 b, float c) {
  return __builtin_amdgcn_fdot2_f32_bf16(__builtin_bit_cast(bf16x2, a), __builtin_bit_cast(bf16x2, b), c, false);
}
DI float cosr(float rev) { return __builtin_amdgcn_cosf(rev); }
DI float sinr(float rev) { return __builtin_amdgcn_sinf(rev); }
template <int CTRL, int RM> DI u32 dppu(u32 v) { return (u32)__builtin_amdgcn_update_dpp(0, (int)v, CTRL, RM, 0xf, true); }
template <int CTRL, int RM> DI float dpp_add(float v) { return v + asf(dppu<CTRL, RM>(asu(v))); }
DI float wave_sum(float v) {
  v = dpp_add<0xB1, 0xf>(v); v = dpp_add<0x4E, 0xf>(v); v = dpp_add<0x141, 0xf>(v);
  v = dpp_add<0x140, 0xf>(v); v = dpp_add<0x142, 0xa>(v); v = dpp_add<0x143, 0xc>(v);
  return asf(__builtin_amdgcn_readlane(asu(v), 63));
}
DI u32 wave_umax(u32 v) {
  v = max(v, dppu<0xB1, 0xf>(v)); v = max(v, dppu<0x4E, 0xf>(v)); v = max(v, dppu<0x141, 0xf>(v));
  v = max(v, dppu<0x140, 0xf>(v)); v = max(v, dppu<0x142, 0xa>(v)); v = max(v, dppu<0x143, 0xc>(v));
  return __builtin_amdgcn_readlane(v, 63);
}
DI float sigm(float x) { return __builtin_amdgcn_rcpf(1.f + __expf(-x)); }
DI u32 umax4(u32x4 v) { return max(max(v[0], v[1]), max(v[2], v[3])); }

DI u32x4 ld16(rsrc_t r, u32 o) { return __builtin_amdgcn_raw_buffer_load_b128(r, o, 0, 16); }
DI u32x2 ld8(rsrc_t r, u32 o) { return __builtin_amdgcn_raw_buffer_load_b64(r, o, 0, 16); }
DI u32 ld4(rsrc_t r, u32 o) { return __builtin_amdgcn_raw_buffer_load_b32(r, o, 0, 16); }
DI void st16(rsrc_t r, u32 o, u32x4 v) { __builtin_amdgcn_raw_buffer_store_b128(v, r, o, 0, 16); }
DI void st8(rsrc_t r, u32 o, u32x2 v) { __builtin_amdgcn_raw_buffer_store_b64(v, r, o, 0, 16); }
DI void st4(rsrc_t r, u32 o, u32 v) { __builtin_amdgcn_raw_buffer_store_b32(v, r, o, 0, 16); }
#define DRAIN() asm volatile("s_waitcnt vmcnt(0)" ::: "memory")
#define BAR() asm volatile("s_waitcnt lgkmcnt(0)\n\ts_barrier" ::: "memory")
#define REREAD() asm volatile("" ::: "memory")
#define PIN() __builtin_amdgcn_sched_barrier(0)
template <class T> DI void keep(T& v) { asm volatile("" : "+v"(v)); }
DI void glds16(const void* g, u32 lds_byte) {
  u32 m;
  asm volatile("s_mov_b32 %0, m0\n\ts_mov_b32 m0, %2\n\ts_nop 0\n\tglobal_load_lds_dwordx4 %1, off\n\ts_mov_b32 m0, %0"
               : "=&s"(m) : "v"(g), "s"(lds_byte) : "memory");
}
#define STAMP(i)
#define STAMP_OUT(role)

#define SLEEP_NC 30
#define SLEEP_A 12
#define SLEEP_C 30

__global__ __launch_bounds__(768, 3) void fused(const u16* __restrict__ x, const u16* __restrict__ fwr, const u16* __restrict__ fwi,
                                                const u16* __restrict__ wrt, const u16* __restrict__ wg, const u16* __restrict__ wu,
                                                const u16* __restrict__ wd, const u16* __restrict__ cvec, const u16* __restrict__ wmod,
                                                u16* out, char* ws) {
  __shared__ __attribute__((aligned(16))) u32 L[L_END];
  const int b = blockIdx.x, xc = b & 7, j = b >> 3, t = threadIdx.x, l = t & 63;
  const int w = __builtin_amdgcn_readfirstlane(t >> 6);
  const int s2 = xc * 32 + j;
  const bool col = j < 21;
  const int seg = xc * 21 + j;
  const rsrc_t rs = __builtin_amdgcn_make_buffer_rsrc(ws, 0, WS_USED, 0x00020000);
  float* Lf = (float*)L;
  STAMP(0);
  u64 code_ptr;
  u32 code_len;
  asm volatile("s_getpc_b64 %0\n.Lcode_from:\n\ts_mov_b32 %1, .Lcode_to-.Lcode_from-4\n\t.subsection 1\n.Lcode_to:\n\t.subsection 0" : "=s"(code_ptr), "=s"(code_len));

  if (w >= 4) {
    const int rw = w - 4, tr = t - 256;
    const u16* xrow = x + (size_t)(s2 + 256 * rw) * D;
    const int mb = j < 10 ? xc * 10 + j : 80 + xc * 20 + (j - 10);
    const bool hasmod = j < 30;
    u32x4 wm[3] = {};
    float cv[3] = {};
    if (hasmod) {
      const u16* wp = wmod + (size_t)(tr >> 1) * 3840 + mb * 16 + (tr & 1) * 8;
      wm[0] = *(const u32x4*)wp; wm[1] = *(const u32x4*)(wp + 256 * 3840);
      cv[0] = ldbf(cvec + (tr >> 1)); cv[1] = ldbf(cvec + 256 + (tr >> 1));
      if (rw < 4) { wm[2] = *(const u32x4*)(wp + 512 * 3840); cv[2] = ldbf(cvec + 512 + (tr >> 1)); }
    }
    const u32x2 xa = *(const u32x2*)(xrow + 4 * l), xb = *(const u32x2*)(xrow + 256 + 4 * l);
    const u32 xd = *(const u32*)(xrow + 512 + 2 * l);
    const u32x2 ep = ld8(rs, O_EPOCH);
    PIN();
    if (hasmod) {
      float acc[8] = {};
#pragma unroll
      for (int i = 0; i < 3; ++i) {
        const float sc = bfr(cv[i] * sigm(cv[i]));
#pragma unroll
        for (int q = 0; q < 4; ++q) { acc[2 * q] += sc * lo16(wm[i][q]); acc[2 * q + 1] += sc * hi16(wm[i][q]); }
      }
#pragma unroll
      for (int q = 0; q < 8; ++q) { acc[q] = dpp_add<0x4E, 0xf>(acc[q]); acc[q] = dpp_add<0x124, 0xf>(acc[q]); acc[q] = dpp_add<0x128, 0xf>(acc[q]); }
      if ((l & 14) == 0) {
        float* p = Lf + L_R + (tr >> 4) * 16 + (l & 1) * 8;
        *(f32x4*)p = (f32x4){acc[0], acc[1], acc[2], acc[3]};
        *(f32x4*)(p + 4) = (f32x4){acc[4], acc[5], acc[6], acc[7]};
      }
    }
    float xv[10] = {lo16(xa[0]), hi16(xa[0]), lo16(xa[1]), hi16(xa[1]), lo16(xb[0]), hi16(xb[0]), lo16(xb[1]), hi16(xb[1]), lo16(xd), hi16(xd)};
    {
      float s = 0, q = 0;
#pragma unroll
      for (int i = 0; i < 10; ++i) { s += xv[i]; q += xv[i] * xv[i]; }
      s = wave_sum(s); q = wave_sum(q);
      const float m = s * (1.f / D), rstd = __builtin_amdgcn_rsqf(fmaxf(q * (1.f / D) - m * m, 0.f) + EPS);
      float* z = Lf + L_Z + rw * 640;
      *(f32x4*)(z + 4 * l) = (f32x4){(xv[0] - m) * rstd, (xv[1] - m) * rstd, (xv[2] - m) * rstd, (xv[3] - m) * rstd};
      *(f32x4*)(z + 256 + 4 * l) = (f32x4){(xv[4] - m) * rstd, (xv[5] - m) * rstd, (xv[6] - m) * rstd, (xv[7] - m) * rstd};
      *(f32x2*)(z + 512 + 2 * l) = (f32x2){(xv[8] - m) * rstd, (xv[9] - m) * rstd};
    }
    const u64 tag = (((u64)ep[1] << 32) | ep[0]) + 1;
    u32 tlo = (u32)tag, thi = (u32)(tag >> 32);
    keep(tlo); keep(thi);
    STAMP(1);
    BAR();
    if (rw == 7) {
      if (hasmod) {
        float s = 0;
#pragma unroll
        for (int i = 0; i < 8; ++i) s += Lf[L_R + ((l >> 4) * 8 + i) * 16 + (l & 15)];
        auto a = __builtin_amdgcn_permlane32_swap(asu(s), asu(s), false, false);
        s = asf(a[0]) + asf(a[1]);
        auto c = __builtin_amdgcn_permlane16_swap(asu(s), asu(s), false, false);
        s = asf(c[0]) + asf(c[1]);
        const float o = asf(dppu<0xB1, 0xf>(asu(s)));
        const u32 v = (l & 1) ? pk(o, s) : pk(s, o);
        st8(rs, O_MOD + ((l >> 4) + 4 * (l & 1)) * MOD_REP + mb * 64 + ((l & 15) >> 1) * 8, (u32x2){v, (l & 2) ? thi : tlo});
      }
    }
    STAMP(2);
    if (rw < 5) {
      f32x2 z[8];
#pragma unroll
      for (int i = 0; i < 8; ++i) z[i] = *(const f32x2*)(Lf + L_Z + i * 640 + 2 * tr);
      const f32x2 a0 = z[0] + z[4], a1 = z[0] - z[4], b0 = z[2] + z[6], b1 = z[2] - z[6];
      const f32x2 c0 = z[1] + z[5], c1 = z[1] - z[5], d0 = z[3] + z[7], d1 = z[3] - z[7];
      const f32x2 e = a0 + b0, f = c0 + d0, p = (c1 - d1) * 0.70710678f, q = (c1 + d1) * 0.70710678f;
      f32x2 re[4] = {e + f, a1 + p, a0 - b0, a1 - p};
      f32x2 im[4] = {e - f, -b1 - q, d0 - c0, b1 - q};
#pragma unroll
      for (int g = 0; g < 4; ++g) { keep(re[g]); keep(im[g]); }
      const u32 mo = O_MOD + xc * MOD_REP + tr * 8, want = (l & 1) ? thi : tlo;
      u32x2 gs, gc;
#pragma nounroll
      for (int sp = 0; sp < SPIN; ++sp) {
        REREAD();
        gs = ld8(rs, mo); gc = ld8(rs, mo + 2560);
        if (!__any(((gs[1] ^ want) | (gc[1] ^ want)) != 0)) break;
      }
      STAMP(3);
      const f32x2 A = {bfr(1.f + lo16(gc[0])), bfr(1.f + hi16(gc[0]))}, sa8 = {8.f * lo16(gs[0]), 8.f * hi16(gs[0])};
      u32* u = L + L_U + (tr & 15) * 20 + (tr >> 4);
#pragma unroll
      for (int g = 0; g < 4; ++g) {
        const f32x2 r = g == 0 ? re[g] * A + sa8 : re[g] * A, i = im[g] * A;
        u[g * 640] = pk(r[0], i[0]);
        u[g * 640 + 320] = pk(r[1], i[1]);
      }
    }
    BAR();
    STAMP(4);
    int k1 = 0, kap0 = 0, lam0 = 0;
    if (col) {
      if (seg < 48) { const int c0 = (seg % 6) * 4; k1 = c0 >= 12 ? 4 : 0; kap0 = c0 >= 12 ? c0 - 12 : c0; lam0 = 4 * (seg / 6); }
      else { k1 = 1 + (seg - 48) / 40; kap0 = ((seg - 48) % 5) * 4; lam0 = 4 * (((seg - 48) % 40) / 5); }
      const bool mir = lam0 >= 16;
      const int kd = kap0 + 20 * (lam0 + (tr & 3)), cw = mir ? D - 3 - kd : kd;
      u32x2 a[2], c[2];
#pragma unroll
      for (int i = 0; i < 2; ++i) {
        const int ks = k1 + 8 * ((tr >> 2) + 128 * i), o = (mir ? (S - ks) & (S - 1) : ks) * DF + cw;
        a[i] = *(const u32x2u*)(fwr + o); c[i] = *(const u32x2u*)(fwi + o);
      }
#pragma unroll
      for (int i = 0; i < 2; ++i) {
        u32x4 v = {__builtin_amdgcn_perm(c[i][0], a[i][0], 0x05040100), __builtin_amdgcn_perm(c[i][0], a[i][0], 0x07060302),
                   __builtin_amdgcn_perm(c[i][1], a[i][1], 0x05040100), __builtin_amdgcn_perm(c[i][1], a[i][1], 0x07060302)};
        if (mir) v = (u32x4){v[3] ^ 0x80000000u, v[2] ^ 0x80000000u, v[1] ^ 0x80000000u, v[0] ^ 0x80000000u};
        *(u32x4*)(L + L_W + (tr + 512 * i) * 4) = v;
      }
    }
    {
      const u32 mo = O_MOD + xc * MOD_REP + 5120 + tr * 16, mo2 = rw < 2 ? mo + 8192 : (rw < 4 ? O_TOK + (tr - 128) * 16 : NONE);
      u32x4 g0, g1;
#pragma nounroll
      for (int sp = 0; sp < SPIN; ++sp) {
        REREAD();
        g0 = ld16(rs, mo); g1 = ld16(rs, mo2);
        u32 bad = (g0[1] ^ tlo) | (g0[3] ^ thi);
        if (rw < 2) bad |= (g1[1] ^ tlo) | (g1[3] ^ thi);
        else if (rw < 4) bad |= (g1[0] ^ tlo) | (g1[1] ^ thi) | (g1[2] ^ tlo) | (g1[3] ^ thi);
        if (!__any(bad != 0)) break;
        __builtin_amdgcn_s_sleep(4);
      }
      *(u32x2*)(L + L_M + tr * 2) = (u32x2){g0[0], g0[2]};
      if (rw < 2) *(u32x2*)(L + L_M + 1024 + tr * 2) = (u32x2){g1[0], g1[2]};
    }
    const int m16 = l & 15, gam = l >> 4;
    u32x4 Fr, Fi, Gr, Gi;
    float tc[4], ts[4];
#pragma unroll
    for (int q = 0; q < 4; ++q) {
      const float e = (float)(m16 * (4 * gam + q));
      const float a = e * (1.f / 16.f), cs = cosr(a), sn = sinr(a);
      Fr[q] = pk(cs, sn); Fi[q] = pk(-sn, cs); Gr[q] = pk(cs, -sn); Gi[q] = pk(sn, cs);
      tc[q] = cosr(e * (1.f / 256.f)); ts[q] = sinr(e * (1.f / 256.f));
    }
    keep(Fr); keep(Fi); keep(Gr); keep(Gi);
#pragma unroll
    for (int q = 0; q < 4; ++q) { keep(tc[q]); keep(ts[q]); }
    STAMP(5);
    BAR();
    STAMP(6);
    if (b == 0 && tr == 0) st8(rs, O_EPOCH, (u32x2){tlo, thi});
    if (col) {
      const u32 o = O_T1 + seg * SEGB + tr * 16;
      u32x4 v0 = ld16(rs, o), v1 = ld16(rs, o + 8192);
      const int c = rw >> 1, lp = (rw & 1) * 2;
      float fr[2][4], fi[2][4];
#pragma unroll
      for (int u = 0; u < 2; ++u)
#pragma unroll
        for (int r = 0; r < 4; ++r) {
          const u32 v = L[L_W + (m16 + 16 * (4 * gam + r)) * 16 + (lp + u) * 4 + c];
          fr[u][r] = lo16(v); fi[u][r] = hi16(v);
        }
      if (rw == 0 && kap0 == 0 && (lam0 & 15) == 0) {
        const bool mir = lam0 != 0;
#pragma unroll
        for (int r = 0; r < 4; ++r) {
          const int ks = k1 + 8 * (m16 + 16 * (4 * gam + r)), o2 = (mir ? ks : (S - ks) & (S - 1)) * DF + 20 * lam0;
          const float wi2 = ldbf(fwi + o2);
          fr[0][r] = 0.5f * (fr[0][r] + ldbf(fwr + o2));
          fi[0][r] = 0.5f * (fi[0][r] + (mir ? wi2 : -wi2));
        }
      }
#pragma unroll
      for (int u = 0; u < 2; ++u)
#pragma unroll
        for (int r = 0; r < 4; ++r) { keep(fr[u][r]); keep(fi[u][r]); }
#pragma nounroll
      for (int sp = 0; sp < SPIN && __any(max(umax4(v0), umax4(v1)) == SENT); ++sp) {
        REREAD();
        v0 = ld16(rs, o); v1 = ld16(rs, o + 8192);
      }
      STAMP(7);
      u32* lx = L + L_X + (tr >> 2) * 16 + (((tr ^ (tr >> 4)) & 3) << 2);
      *(u32x4*)lx = v0; *(u32x4*)(lx + 2048) = v1;
      BAR();
      u32* lf = L + L_X + gam * 1024 + m16 * 16 + ((c ^ (m16 >> 2)) << 2) + lp;
      u32x2 io[4];
#pragma unroll
      for (int q = 0; q < 4; ++q) io[q] = *(const u32x2*)(lf + q * 256);
#pragma unroll
      for (int u = 0; u < 2; ++u) {
        const f32x4 z = {0, 0, 0, 0};
        u32x4 a = {io[0][u], io[1][u], io[2][u], io[3][u]};
        f32x4 r = mfma16(a, Fr, z), i = mfma16(a, Fi, z);
#pragma unroll
        for (int q = 0; q < 4; ++q) a[q] = pk(r[q] * tc[q] + i[q] * ts[q], i[q] * tc[q] - r[q] * ts[q]);
        r = mfma16(Fr, a, z); i = mfma16(Fi, a, z);
#pragma unroll
        for (int q = 0; q < 4; ++q) a[q] = pk(r[q] * fr[u][q] - i[q] * fi[u][q], r[q] * fi[u][q] + i[q] * fr[u][q]);
        r = mfma16(a, Gr, z); i = mfma16(a, Gi, z);
#pragma unroll
        for (int q = 0; q < 4; ++q) a[q] = pk(r[q] * tc[q] - i[q] * ts[q], i[q] * tc[q] + r[q] * ts[q]);
        r = mfma16(Gr, a, z); i = mfma16(Gi, a, z);
#pragma unroll
        for (int q = 0; q < 4; ++q) io[q][u] = pk(r[q], i[q]);
      }
#pragma unroll
      for (int q = 0; q < 4; ++q) *(u32x2*)(lf + q * 256) = io[q];
      BAR();
      st16(rs, o + (O_T2 - O_T1), *(const u32x4*)lx);
      st16(rs, o + (O_T2 - O_T1) + 8192, *(const u32x4*)(lx + 2048));
      STAMP(8);
      DRAIN();
      STAMP(9);
      BAR();
    }
    float ga[10], cb[10], sb[10], gb[10], vv[10];
    float sv = 0, sbw = 0;
    {
      auto ld10 = [&](const u32* p, float (&v)[10]) {
        const u32x2 a = *(const u32x2*)(p + 2 * l), c = *(const u32x2*)(p + 128 + 2 * l);
        const u32 e = p[256 + l];
        v[0] = lo16(a[0]); v[1] = hi16(a[0]); v[2] = lo16(a[1]); v[3] = hi16(a[1]);
        v[4] = lo16(c[0]); v[5] = hi16(c[0]); v[6] = lo16(c[1]); v[7] = hi16(c[1]); v[8] = lo16(e); v[9] = hi16(e);
      };
      ld10((const u32*)wrt, vv);
      ld10(L + L_M, ga); ld10(L + L_M + 320, sb); ld10(L + L_M + 640, cb); ld10(L + L_M + 960, gb);
#pragma unroll
      for (int i = 0; i < 10; ++i) {
        cb[i] = bfr(1.f + cb[i]);
        sbw += sb[i] * vv[i];
        vv[i] *= cb[i];
        sv += vv[i];
        ga[i] *= 1.f / ((float)S * (float)D);
      }
      sv = wave_sum(sv); sbw = wave_sum(sbw);
    }
    u32 cf[4];
    cf[0] = pk(1.f, (rw & 1) ? -1.f : 1.f);
#pragma unroll
    for (int g = 1; g < 4; ++g) { const float th = (float)(rw * g) * 0.125f; cf[g] = pk(2.f * cosr(th), -2.f * sinr(th)); }
#pragma unroll
    for (int i = 0; i < 10; ++i) { keep(ga[i]); keep(cb[i]); keep(sb[i]); keep(gb[i]); keep(vv[i]); keep(xv[i]); }
#pragma unroll
    for (int g = 0; g < 4; ++g) keep(cf[g]);
    STAMP(10);
    BAR();
    STAMP(11);
    float y[10] = {};
#pragma unroll
    for (int g = 0; g < 4; ++g) {
      const u32* e = L + L_U + g * 640;
      const u32x4 a = *(const u32x4*)(e + 4 * l), c = *(const u32x4*)(e + 256 + 4 * l);
      const u32x2 f = *(const u32x2*)(e + 512 + 2 * l);
#pragma unroll
      for (int q = 0; q < 4; ++q) { y[q] = dot2(a[q], cf[g], y[q]); y[4 + q] = dot2(c[q], cf[g], y[4 + q]); }
      y[8] = dot2(f[0], cf[g], y[8]); y[9] = dot2(f[1], cf[g], y[9]);
    }
    float s = 0, q2 = 0, dt = 0;
#pragma unroll
    for (int i = 0; i < 10; ++i) { xv[i] += ga[i] * y[i]; s += xv[i]; q2 += xv[i] * xv[i]; dt += xv[i] * vv[i]; }
    s = wave_sum(s); q2 = wave_sum(q2); dt = wave_sum(dt);
    const float m = s * (1.f / D), rstd = __builtin_amdgcn_rsqf(fmaxf(q2 * (1.f / D) - m * m, 0.f) + EPS);
    const u32 rb = asu(rstd * (dt - m * sv) + sbw);
    const u32 key = (min((rb & 0x80000000u) ? ~rb : (rb | 0x80000000u), 0xFFFFFFF0u) & ~7u) | (u32)rw;
    if (l == 0) L[L_Q + rw] = key;
    PIN();
    BAR();
    STAMP(12);
    const bool best = max(umax4(*(const u32x4*)(L + L_Q)), umax4(*(const u32x4*)(L + L_Q + 4))) == key;
    float h2[10];
#pragma unroll
    for (int i = 0; i < 10; ++i) { h2[i] = (xv[i] - m) * rstd * cb[i] + sb[i]; xv[i] += gb[i] * h2[i]; }
    const u32x2 oa = {pk(xv[0], xv[1]), pk(xv[2], xv[3])}, ob = {pk(xv[4], xv[5]), pk(xv[6], xv[7])};
    const u32 oc = pk(xv[8], xv[9]);
    if (__builtin_amdgcn_readfirstlane(best)) {
      const u32 ho = O_H2 + s2 * 1280;
      st8(rs, ho + 8 * l, (u32x2){pk(h2[0], h2[1]), pk(h2[2], h2[3])});
      st8(rs, ho + 512 + 8 * l, (u32x2){pk(h2[4], h2[5]), pk(h2[6], h2[7])});
      st4(rs, ho + 1024 + 4 * l, pk(h2[8], h2[9]));
      const rsrc_t ro = __builtin_amdgcn_make_buffer_rsrc(out, 0, S * D * 2, 0x00020000);
      const u32 oo = (s2 + 256 * rw) * 1280;
      st8(ro, oo + 8 * l, oa); st8(ro, oo + 512 + 8 * l, ob); st4(ro, oo + 1024 + 4 * l, oc);
      DRAIN();
      if (l == 0) st4(rs, O_DONE + s2 * 16, 1u);
    } else {
      u16* orow = out + (size_t)(s2 + 256 * rw) * D;
      *(u32x2*)(orow + 4 * l) = oa; *(u32x2*)(orow + 256 + 4 * l) = ob; *(u32*)(orow + 512 + 2 * l) = oc;
    }
    STAMP(13);
    STAMP_OUT(1);
    return;
  }

  const int g = w, c = l & 31, h = l >> 5;
  const int nsg = g == 0 ? 6 : 5;
  const u32 slot = c < 4 * nsg ? ((g == 0 ? 0 : 8 + 40 * g) + h * nsg + (c >> 2)) * SEGB + s2 * 64 + (c & 3) * 16 : NONE;
  const u32 sstep = c < 4 * nsg ? 2 * nsg * SEGB : 0;
  const bool gu = j < 24, dn = j >= 27;
  const int gui = xc * 24 + j, dni = xc * 5 + j - 27;
  {
    u32 pf = 0;
    if (t * 128u < code_len) pf = *(const __attribute__((address_space(1))) volatile u32*)(code_ptr + t * 128u);
    const u32x2 ep = ld8(rs, O_EPOCH);
    const u32x4 sent = {SENT, SENT, SENT, SENT};
#pragma unroll
    for (int i = 0; i < 4; ++i) st16(rs, (c < 4 * nsg ? O_T1 : 0) + slot + i * sstep, sent);
    if (col)
#pragma unroll
      for (int i = 0; i < 4; ++i) st16(rs, O_T2 + seg * SEGB + (t + 256 * i) * 16, sent);
    u32 so = NONE;
    if (t < 80) so = O_H2 + s2 * 1280 + t * 16;
    else if (t < 88) so = O_CAND + (t - 80) * CAND_REP + s2 * 16;
    else if (t < 96) so = gu ? O_ACT + (t - 88) * ACT_REP + gui * 16 : NONE;
    else if (t == 96) so = O_DONE + s2 * 16;
    if (w < 2) st16(rs, so, sent);
    const float kapf = (float)((g == 0 && c >= 12) ? c - 12 : c);
    const float k1f = g ? (float)g : (c >= 12 ? 4.f : 0.f);
    const bool ok = g ? c < 20 : (c < 23 && c != 11);
    const float m0 = ok && (g || c < 12) ? 1.f : 0.f, m1 = ok && (g || c >= 12) ? 1.f : 0.f;
    u32x4 br[3], bi[3], ar[4], ai[4];
    float tc[16], ts[16];
#pragma unroll
    for (int tau = 0; tau < 3; ++tau)
#pragma unroll
      for (int q = 0; q < 4; ++q) {
        const float th = (float)(8 * tau + 4 * h + q) * (kapf * 0.05f), z = (tau == 2 && h) ? 0.f : 1.f;
        const float cs = cosr(th) * z, sn = sinr(th) * z;
        br[tau][q] = pk(cs * m0, (g ? sn : cs) * m1);
        bi[tau][q] = pk(-sn * m0, (g ? cs : -sn) * m1);
      }
#pragma unroll
    for (int tau = 0; tau < 4; ++tau)
#pragma unroll
      for (int q = 0; q < 4; ++q) {
        const float n2 = (float)(16 * (tau & 1) + 8 * h + 2 * q + (tau >> 1));
        const float th = n2 * (kapf * (1.f / 640.f)) + (float)s2 * k1f * (1.f / 2048.f);
        tc[4 * tau + q] = cosr(th); ts[4 * tau + q] = sinr(th);
        const float t2 = n2 * ((float)c * (1.f / 32.f)), c2 = cosr(t2), s2_ = sinr(t2);
        ar[tau][q] = pk(c2, s2_); ai[tau][q] = pk(-s2_, c2);
      }
#pragma unroll
    for (int i = 0; i < 3; ++i) { keep(br[i]); keep(bi[i]); }
#pragma unroll
    for (int i = 0; i < 4; ++i) { keep(ar[i]); keep(ai[i]); }
#pragma unroll
    for (int i = 0; i < 16; ++i) { keep(tc[i]); keep(ts[i]); }
    keep(pf);
    STAMP(1);
    DRAIN();
    STAMP(2);
    BAR();
    if (t == 0) { const u64 tag = (((u64)ep[1] << 32) | ep[0]) + 1; st8(rs, O_TOK + b * 8, (u32x2){(u32)tag, (u32)(tag >> 32)}); }
    const u32* u = L + L_U + g * 640 + c * 20;
    BAR();
    STAMP(4);
    f32x16 yr = {}, yi = {};
#pragma unroll
    for (int tau = 0; tau < 3; ++tau) {
      const u32x4 a = *(const u32x4*)(u + (tau == 2 ? 16 : 8 * tau + 4 * h));
      yr = mfma32(a, br[tau], yr);
      yi = mfma32(a, bi[tau], yi);
    }
    f32x16 zr = {}, zi = {};
#pragma unroll
    for (int tau = 0; tau < 4; ++tau) {
      u32x4 v;
#pragma unroll
      for (int q = 0; q < 4; ++q) {
        const int i = 4 * tau + q;
        v[q] = pk(yr[i] * tc[i] + yi[i] * ts[i], yi[i] * tc[i] - yr[i] * ts[i]);
      }
      zr = mfma32(ar[tau], v, zr);
      zi = mfma32(ai[tau], v, zi);
    }
#pragma unroll
    for (int i = 0; i < 4; ++i)
      st16(rs, (c < 4 * nsg ? O_T1 : 0) + slot + i * sstep,
           (u32x4){pk(zr[4 * i], zi[4 * i]), pk(zr[4 * i + 1], zi[4 * i + 1]), pk(zr[4 * i + 2], zi[4 * i + 2]), pk(zr[4 * i + 3], zi[4 * i + 3])});
    STAMP(5);
    DRAIN();
    STAMP(6);
    BAR();
  }
  {
    u32x4 br[4], bi[4], ar[3], ai[3];
    float tc[12], ts[12];
    const float cf = (float)c;
#pragma unroll
    for (int tt = 0; tt < 4; ++tt)
#pragma unroll
      for (int q = 0; q < 4; ++q) {
        const float th = (float)(8 * tt + 4 * h + q) * (cf * (1.f / 32.f)), cs = cosr(th), sn = sinr(th);
        br[tt][q] = pk(cs, -sn); bi[tt][q] = pk(sn, cs);
      }
#pragma unroll
    for (int tau = 0; tau < 3; ++tau)
#pragma unroll
      for (int q = 0; q < 4; ++q) {
        const int cc = 8 * tau + 4 * h + q;
        const bool up = g == 0 && cc >= 12;
        const int kap = up ? cc - 12 : cc;
        const float kapf = (float)kap, k1f = g ? (float)g : (up ? 4.f : 0.f);
        const float th = kapf * (cf * (1.f / 640.f)) + (float)s2 * k1f * (1.f / 2048.f);
        tc[4 * tau + q] = cosr(th); ts[4 * tau + q] = sinr(th);
        const float wt = g ? (cc < 20 ? 1.f : 0.f) : (kap == 11 ? 0.f : ((kap == 0 || kap == 10) ? 1.f : 2.f));
        const float t2 = kapf * (cf * 0.05f), c2 = cosr(t2) * wt, s2_ = sinr(t2) * wt;
        const u32 p = pk(c2, -s2_);
        ar[tau][q] = up ? 0u : p;
        ai[tau][q] = g ? pk(s2_, c2) : (up ? p : 0u);
      }
#pragma unroll
    for (int i = 0; i < 4; ++i) { keep(br[i]); keep(bi[i]); }
#pragma unroll
    for (int i = 0; i < 3; ++i) { keep(ar[i]); keep(ai[i]); }
#pragma unroll
    for (int i = 0; i < 12; ++i) { keep(tc[i]); keep(ts[i]); }
    STAMP(7);
    const u32 so = (c < 4 * nsg ? O_T2 : 0) + slot;
    if (col) {
      BAR();
    } else {
      __builtin_amdgcn_s_sleep(8);
    }
    if (gu || dn) {
      const int n = gu ? 5 : 12;
      const size_t stride = gu ? F * 2 : D * 2;
      const char* p = gu ? (const char*)(((l & 1) ? wu : wg) + gui * 8) : (const char*)(wd + dni * 16 + (l & 1) * 8);
      p += (size_t)(g * n * 32 + (l >> 1)) * stride;
      const u32 lb = (u32)(size_t)(LAS u32*)(L + L_T) + g * 12288;
#pragma nounroll
      for (int i = 0; i < n; ++i) glds16(p + (size_t)i * 32 * stride, lb + i * 1024);
    }
    if (col) {
      BAR();
      BAR();
    } else {
      __builtin_amdgcn_s_sleep(SLEEP_NC);
      const u32 co = l == 0 ? so + 3 * sstep : NONE;
#pragma nounroll
      for (int sp = 0; sp < SPIN; ++sp) {
        REREAD();
        if (!__any(ld4(rs, co) == SENT)) break;
        __builtin_amdgcn_s_sleep(2);
      }
    }
    STAMP(8);
    u32x4 a[4];
#pragma nounroll
    for (int sp = 0; sp < SPIN; ++sp) {
      REREAD();
#pragma unroll
      for (int i = 0; i < 4; ++i) a[i] = ld16(rs, so + i * sstep);
      if (!__any(max(max(umax4(a[0]), umax4(a[1])), max(umax4(a[2]), umax4(a[3]))) == SENT)) break;
    }
    STAMP(9);
    f32x16 yr = {}, yi = {};
#pragma unroll
    for (int i = 0; i < 4; ++i) { yr = mfma32(a[i], br[i], yr); yi = mfma32(a[i], bi[i], yi); }
    f32x16 zr = {}, zi = {};
#pragma unroll
    for (int tau = 0; tau < 3; ++tau) {
      u32x4 v;
#pragma unroll
      for (int q = 0; q < 4; ++q) {
        const int i = 4 * tau + q;
        v[q] = pk(yr[i] * tc[i] - yi[i] * ts[i], yi[i] * tc[i] + yr[i] * ts[i]);
      }
      zr = mfma32(ar[tau], v, zr);
      zi = mfma32(ai[tau], v, zi);
    }
    u32* e = L + L_U + g * 640 + h * 128 + c;
#pragma unroll
    for (int r = 0; r < 8; ++r) e[(r >> 2) * 256 + (r & 3) * 32] = pk(zr[r], zi[r]);
    if (h == 0)
#pragma unroll
      for (int r = 8; r < 12; ++r) e[512 + (r & 3) * 32] = pk(zr[r], zi[r]);
    STAMP(10);
    BAR();
  }
  const int m16 = l & 15, gam = l >> 4;
  u32x4 wf[12];
  {
    const LAS u32* p = (const LAS u32*)(L + L_T) + g * 3072 + gam * 64 + (m16 >> 2) * 8 + (m16 & 3) * 2;
#pragma unroll
    for (int i = 0; i < 12; ++i) {
      if (i < 5 || dn) {
        const u32x2 lo = __builtin_bit_cast(u32x2, __builtin_amdgcn_ds_read_tr16_b64_v4i16((LAS s16x4*)(p + i * 256)));
        const u32x2 hi = __builtin_bit_cast(u32x2, __builtin_amdgcn_ds_read_tr16_b64_v4i16((LAS s16x4*)(p + i * 256 + 32)));
        wf[i] = (u32x4){lo[0], lo[1], hi[0], hi[1]};
      } else wf[i] = (u32x4){0, 0, 0, 0};
      keep(wf[i]);
    }
  }
  BAR();
  STAMP(11);
  if (w == 0) {
    const u32 key = max(umax4(*(const u32x4*)(L + L_Q)), umax4(*(const u32x4*)(L + L_Q + 4)));
    st4(rs, l < 8 ? O_CAND + l * CAND_REP + s2 * 16 : NONE, key);
  }
  if (!gu && !dn) { STAMP_OUT(0); return; }
  __builtin_amdgcn_s_sleep(SLEEP_A);
  u32x4 ck;
  const u32 co = O_CAND + xc * CAND_REP + l * 16;
#pragma nounroll
  for (int sp = 0; sp < SPIN; ++sp) {
    REREAD();
#pragma unroll
    for (int i = 0; i < 4; ++i) ck[i] = ld4(rs, co + i * 1024);
    if (!__any(umax4(ck) == SENT)) break;
  }
  const u32 kmax = wave_umax(umax4(ck));
  const int wi_ = ck[0] == kmax ? 0 : (ck[1] == kmax ? 64 : (ck[2] == kmax ? 128 : 192));
  const int wl = __builtin_ctzll(__ballot(umax4(ck) == kmax));
  const int win = __builtin_amdgcn_readlane(wi_, wl) + wl;
  STAMP(12);
  const f32x4 z4 = {0, 0, 0, 0};
  if (gu) {
    const u32 ho = m16 < 5 ? O_H2 + win * 1280 + (5 * g + m16) * 64 + gam * 16 : NONE;
    u32x4 a;
#pragma nounroll
    for (int sp = 0; sp < SPIN; ++sp) {
      REREAD();
      a = ld16(rs, ho);
      if (!__any(umax4(a) == SENT)) break;
    }
    STAMP(13);
    const f32x4 d0 = mfma16(a, wf[0], z4), d1 = mfma16(a, wf[1], z4), d2 = mfma16(a, wf[2], z4), d3 = mfma16(a, wf[3], z4), d4 = mfma16(a, wf[4], z4);
    if (l < 32) Lf[L_Q + 16 + g * 32 + l] = gam ? d4[0] : (d0[0] + d1[1]) + (d2[2] + d3[3]);
    BAR();
    if (w == 0) {
      f32x2 gs = {0, 0}, us = {0, 0};
#pragma unroll
      for (int i = 0; i < 8; ++i) {
        gs += *(const f32x2*)(Lf + L_Q + 16 + i * 16 + (l & 3) * 2);
        us += *(const f32x2*)(Lf + L_Q + 24 + i * 16 + (l & 3) * 2);
      }
      const float g0 = bfr(gs[0]), g1 = bfr(gs[1]);
      const u32 v = pk(bfr(g0 * sigm(g0)) * bfr(us[0]), bfr(g1 * sigm(g1)) * bfr(us[1]));
      st4(rs, l < 32 ? O_ACT + (l >> 2) * ACT_REP + gui * 16 + (l & 3) * 4 : NONE, v);
    }
  } else {
    const rsrc_t ro = __builtin_amdgcn_make_buffer_rsrc(out, 0, S * D * 2, 0x00020000);
    const u32 oo = l < 8 ? (win + 256 * (kmax & 7)) * 1280 + dni * 32 + l * 4 : NONE;
    u32 base = 0;
    if (w == 0) {
#pragma nounroll
      for (int sp = 0; sp < SPIN; ++sp) {
        REREAD();
        if (ld4(rs, O_DONE + win * 16) != SENT) break;
      }
      REREAD();
      base = ld4(ro, oo);
      keep(base);
    } else {
      __builtin_amdgcn_s_sleep(SLEEP_C);
    }
    const u32 ao = m16 < 12 ? O_ACT + xc * ACT_REP + (12 * g + m16) * 64 + gam * 16 : NONE;
    u32x4 a;
#pragma nounroll
    for (int sp = 0; sp < SPIN; ++sp) {
      REREAD();
      a = ld16(rs, ao);
      if (!__any(umax4(a) == SENT)) break;
    }
    STAMP(13);
    float s = 0;
#pragma unroll
    for (int r = 0; r < 4; ++r) {
      const f32x4 d0 = mfma16(a, wf[r], z4), d1 = mfma16(a, wf[4 + r], z4), d2 = mfma16(a, wf[8 + r], z4);
      s += gam == 0 ? d0[r] : (gam == 1 ? d1[r] : d2[r]);
    }
    if (l < 48) Lf[L_Q + 16 + g * 48 + l] = s;
    BAR();
    if (w == 0) {
      f32x2 y = {0, 0};
#pragma unroll
      for (int i = 0; i < 12; ++i) y += *(const f32x2*)(Lf + L_Q + 16 + i * 16 + (l & 7) * 2);
      const u32 rb = (kmax & 0x80000000u) ? (kmax & 0x7FFFFFFFu) : ~kmax;
      const float sg = bfr(sigm(bfr(asf(rb))));
      const u32 gbp = L[L_M + 960 + dni * 8 + (l & 7)];
      st4(ro, oo, pk(lo16(base) + lo16(gbp) * (y[0] * sg), hi16(base) + hi16(gbp) * (y[1] * sg)));
    }
  }
  STAMP(14);
  STAMP_OUT(0);
}

extern "C" void kernel_launch(void* const* d_in, const int* in_sizes, int n_in, void* d_out, int out_size, void* d_ws, size_t ws_size,
                              hipStream_t stream) {
  fused<<<256, 768, 0, stream>>>((const u16*)d_in[0], (const u16*)d_in[1], (const u16*)d_in[2], (const u16*)d_in[4], (const u16*)d_in[5],
                                 (const u16*)d_in[6], (const u16*)d_in[7], (const u16*)d_in[8], (const u16*)d_in[9], (u16*)d_out, (char*)d_ws);
}
```

```cpp
#include <hip/hip_runtime.h>
#include <stdint.h>

typedef unsigned u32;
typedef unsigned short u16;
typedef unsigned long long u64;
typedef u32 u32x2 __attribute__((ext_vector_type(2)));
typedef u32 u32x4 __attribute__((ext_vector_type(4)));
typedef u32 u32x2u __attribute__((ext_vector_type(2), aligned(2)));
typedef float f32x2 __attribute__((ext_vector_type(2)));
typedef float f32x4 __attribute__((ext_vector_type(4)));
typedef float f32x16 __attribute__((ext_vector_type(16)));
typedef short s16x4 __attribute__((ext_vector_type(4)));
typedef __bf16 bf16x2 __attribute__((ext_vector_type(2)));
typedef __bf16 bf16x8 __attribute__((ext_vector_type(8)));
typedef __amdgpu_buffer_rsrc_t rsrc_t;
#define DI __device__ __forceinline__
#define LAS __attribute__((address_space(3)))
#define GAS __attribute__((address_space(1)))
#define NWAVES 8

constexpr int S = 2048, D = 640, DF = 321, F = 1536;
constexpr float EPS = 1e-5f;
constexpr u32 SENT = 0xFFFFFFFFu, NONE = 0xFFFFFF00u;
constexpr int SPIN = 1 << 16;

constexpr u32 O_EPOCH = 0;
constexpr u32 O_TOK = 212992, TOK_REP = 2048;
constexpr u32 O_DONE = 8192;
constexpr u32 O_CAND = 16384, CAND_REP = 4096;
constexpr u32 O_ACT = 49152, ACT_REP = 4096;
constexpr u32 O_MOD = 81920, MOD_REP = 16384;
constexpr u32 O_H2 = 262144;
constexpr u32 SEGB = 16384 + 2304;
constexpr u32 O_T = 1u << 20;
constexpr u32 O_STAMP = 7u << 20;
constexpr u32 WS_USED = 8u << 20;

constexpr int L_U = 0;
constexpr int L_M = 2560;
constexpr int L_Q = 3840;
constexpr int L_H = 4352;
constexpr int L_G = 4672;
constexpr int L_T = 14912;
constexpr int L_Z = L_T;
constexpr int L_W = L_G;
constexpr int L_X1 = L_G + 4096;
constexpr int L_W2 = L_G + 8192;
constexpr int L_X2 = L_T + 5120;
constexpr int L_D = 35392;
constexpr int L_END = 35456;

DI u32 pk(float lo, float hi) { return __builtin_bit_cast(u32, __builtin_convertvector((f32x2){lo, hi}, bf16x2)); }
DI float asf(u32 v) { return __builtin_bit_cast(float, v); }
DI u32 asu(float v) { return __builtin_bit_cast(u32, v); }
DI float lo16(u32 v) { return asf(v << 16); }
DI float hi16(u32 v) { return asf(v & 0xffff0000u); }
DI f32x2 up2(u32 v) { return (f32x2){lo16(v), hi16(v)}; }
DI float bfr(float x) { return lo16(pk(x, 0.f)); }
DI float ldbf(const u16* p) { return lo16(*p); }
DI f32x16 mfma32(u32x4 a, u32x4 b, f32x16 c) {
  return __builtin_amdgcn_mfma_f32_32x32x16_bf16(__builtin_bit_cast(bf16x8, a), __builtin_bit_cast(bf16x8, b), c, 0, 0, 0);
}
DI f32x4 mfma16(u32x4 a, u32x4 b, f32x4 c) {
  return __builtin_amdgcn_mfma_f32_16x16x32_bf16(__builtin_bit_cast(bf16x8, a), __builtin_bit_cast(bf16x8, b), c, 0, 0, 0);
}
DI float dot2(u32 a, u32 b, float c) {
  return __builtin_amdgcn_fdot2_f32_bf16(__builtin_bit_cast(bf16x2, a), __builtin_bit_cast(bf16x2, b), c, false);
}
DI float cosr(float rev) { return __builtin_amdgcn_cosf(rev); }
DI float sinr(float rev) { return __builtin_amdgcn_sinf(rev); }
template <int CTRL> DI u32 dppu(u32 v) { return (u32)__builtin_amdgcn_update_dpp(0, (int)v, CTRL, 0xf, 0xf, true); }
template <int CTRL> DI float dppf(float v) { return asf(dppu<CTRL>(asu(v))); }
DI float row_sum(float v) {
  v += dppf<0xB1>(v); v += dppf<0x4E>(v); v += dppf<0x141>(v); v += dppf<0x140>(v);
  return v;
}
DI float fold32(float a, float b) { auto r = __builtin_amdgcn_permlane32_swap(asu(a), asu(b), false, false); return asf(r[0]) + asf(r[1]); }
DI float fold16(float a, float b) { auto r = __builtin_amdgcn_permlane16_swap(asu(a), asu(b), false, false); return asf(r[0]) + asf(r[1]); }
DI float rdl(float v, int lane) { return asf(__builtin_amdgcn_readlane(asu(v), lane)); }
DI float sigm(float x) { return __builtin_amdgcn_rcpf(1.f + __expf(-x)); }
DI u32 umax4(u32x4 v) { return max(max(v[0], v[1]), max(v[2], v[3])); }
template <class V> DI f32x2 pr(const V& v, int i) { return (f32x2){v[i], v[i + 1]}; }
DI void cmul2(f32x2 r, f32x2 im, f32x2 c, f32x2 s, u32& o0, u32& o1) {
  const f32x2 a = r * c - im * s, b = im * c + r * s;
  o0 = pk(a[0], b[0]); o1 = pk(a[1], b[1]);
}
DI u32x4 trfrag(const LAS u32* p, int hi) {
  const u32x2 a = __builtin_bit_cast(u32x2, __builtin_amdgcn_ds_read_tr16_b64_v4i16((LAS s16x4*)p));
  const u32x2 b = __builtin_bit_cast(u32x2, __builtin_amdgcn_ds_read_tr16_b64_v4i16((LAS s16x4*)(p + hi)));
  return (u32x4){a[0], a[1], b[0], b[1]};
}

DI u32x4 ld16(rsrc_t r, u32 o) { return __builtin_amdgcn_raw_buffer_load_b128(r, o, 0, 16); }
DI u32x2 ld8(rsrc_t r, u32 o) { return __builtin_amdgcn_raw_buffer_load_b64(r, o, 0, 16); }
DI u32 ld4(rsrc_t r, u32 o) { return __builtin_amdgcn_raw_buffer_load_b32(r, o, 0, 16); }
DI void st16(rsrc_t r, u32 o, u32x4 v) { __builtin_amdgcn_raw_buffer_store_b128(v, r, o, 0, 16); }
DI void st8(rsrc_t r, u32 o, u32x2 v) { __builtin_amdgcn_raw_buffer_store_b64(v, r, o, 0, 16); }
DI void st4(rsrc_t r, u32 o, u32 v) { __builtin_amdgcn_raw_buffer_store_b32(v, r, o, 0, 16); }
#define DRAIN() asm volatile("s_waitcnt vmcnt(0)" ::: "memory")
#define BAR() do { __builtin_amdgcn_sched_barrier(0); asm volatile("s_waitcnt lgkmcnt(0)\n\ts_barrier" ::: "memory"); __builtin_amdgcn_sched_barrier(0); } while (0)
#define REREAD() asm volatile("" ::: "memory")
#define PIN() __builtin_amdgcn_sched_barrier(0)
#define PRIO(n) __builtin_amdgcn_s_setprio(n)
template <class T> DI void keep(T& v) { asm volatile("" : "+v"(v)); }
DI void glds16(const void* base, u32 off, u32 lds_byte) {
  u32 m;
  asm volatile("s_mov_b32 %0, m0\n\ts_mov_b32 m0, %3\n\ts_nop 0\n\tglobal_load_lds_dwordx4 %1, %2\n\ts_mov_b32 m0, %0"
               : "=&s"(m) : "v"(off), "s"(base), "s"(lds_byte) : "memory");
}
DI void warm(u64 base, u32 bytes, u32 off, u32 lds_byte) {
  const u32x4 d = {(u32)base, (u32)(base >> 32) & 0xffffu, bytes, 0x00020000u};
  u32 m;
  asm volatile("s_mov_b32 %0, m0\n\ts_mov_b32 m0, %3\n\ts_nop 0\n\tbuffer_load_dword %1, %2, 0 offen lds\n\ts_mov_b32 m0, %0"
               : "=&s"(m) : "v"(off), "s"(d), "s"(lds_byte) : "memory");
}
#define STAMP(i)
#define STAMP_OUT()
#define RETRY(v)

#define WFIRST 1
#define SLEEP_M 0
#define SLEEP_T 12
#define SLEEP_M2 16
#define SLEEP_NC 0
#define SLEEP_C 40

__global__ __launch_bounds__(512, 2) void fused(const u16* __restrict__ x, const u16* __restrict__ cvec, const u16* __restrict__ wmod, char* ws,
                                                const u16* __restrict__ fwr, const u16* __restrict__ fwi, const u16* __restrict__ wrt,
                                                const u16* __restrict__ wg, const u16* __restrict__ wu, const u16* __restrict__ wd, u16* out) {
  __shared__ __attribute__((aligned(16))) u32 L[L_END];
  STAMP(0);
  u64 code_ptr;
  u32 code_len;
  asm volatile("s_getpc_b64 %0\n.Lcode_from:\n\ts_mov_b32 %1, .Lcode_to-.Lcode_from-4\n\t.subsection 1\n.Lcode_to:\n\t.subsection 0"
               : "=s"(code_ptr), "=s"(code_len));
  const u32 lds0 = (u32)(size_t)(LAS u32*)L, ldsd = lds0 + L_D * 4;
  warm(code_ptr, code_len, (threadIdx.x * 4 + ((blockIdx.x >> 3) & 3)) * 128u, ldsd);
  asm volatile("" ::"s"(x), "s"(cvec), "s"(wmod), "s"(ws), "s"(fwr), "s"(fwi), "s"(wrt), "s"(wg), "s"(wu), "s"(wd), "s"(out));
  const int b = blockIdx.x, xc = b & 7, j = b >> 3, t = threadIdx.x, l = t & 63;
  const int w = __builtin_amdgcn_readfirstlane(t >> 6), w4 = w & 3;
  const int pos = xc * 32 + j, s2 = 16 * (pos & 15) + (pos >> 4);
  const bool col = j >= 11, hasmod = j < 10;
  const int seg = xc * 21 + j - 11, mb = xc * 10 + j;
  const int m16 = l & 15, gam = l >> 4;
  const rsrc_t rs = __builtin_amdgcn_make_buffer_rsrc(ws, 0, WS_USED, 0x00020000);
  float* Lf = (float*)L;

  u32x2 xa[2], xb[2];
  u32 xd[2];
  u32x4 cf4;
  u64 tag = *(const __attribute__((address_space(4))) u64*)(size_t)(ws + O_EPOCH);
  if (w >= 4) {
#pragma unroll
    for (int i = 0; i < 2; ++i) {
      const u16* xrow = x + (size_t)(s2 + 256 * w4 + 1024 * i) * D;
      xa[i] = *(const u32x2*)(xrow + 4 * l); xb[i] = *(const u32x2*)(xrow + 256 + 4 * l); xd[i] = *(const u32*)(xrow + 512 + 2 * l);
    }
  }
  if (w < 4 && hasmod) {
    asm volatile("global_load_dwordx4 %0, %1, %2" : "=v"(cf4) : "v"(320 * w4 + 64 * min(m16, 4) + 16 * gam), "s"(cvec) : "memory");
    const u32 o = (160 * w4 + (l >> 1)) * 7680 + mb * 32 + (l & 1) * 16;
    int k = mb & 3;
#pragma unroll
    for (int i = 0; i < 5; ++i) { glds16(wmod, o + k * (32 * 7680), lds0 + (L_G + w4 * 2560 + k * 256) * 4); k = k == 4 ? 0 : k + 1; }
  }
  if (w < 2 && col) warm((u64)wmod, 640 * 7680, (t & 3) < 3 ? (32 * (j - 11) + (t >> 2)) * 7680 + 320 * xc + (t & 3) * 156 : NONE, ldsd);
  PIN();
  int k1 = 0, kap0 = 0, lam0 = 0;
  if (col) {
    if (seg < 48) { const int c0 = (seg % 6) * 4; k1 = c0 >= 12 ? 4 : 0; kap0 = c0 >= 12 ? c0 - 12 : c0; lam0 = 4 * (seg / 6); }
    else { k1 = 1 + (seg - 48) / 40; kap0 = ((seg - 48) % 5) * 4; lam0 = 4 * (((seg - 48) % 40) / 5); }
  }
  const bool mir = lam0 >= 16, herm = col && kap0 == 0 && (lam0 & 15) == 0;

  if (w >= 4) {
    const int tr = t - 256;
    u32x4 B1r[4], B1i[4], A4r[4], A4i[4], Fr, Fi, Gr, Gi;
    if (col) {
      const float pf = (float)m16 * (1.f / 256.f), af = (float)(16 * ((m16 >> 2) + 4 * (m16 & 3)) + 4 * w4) * (1.f / 256.f);
#pragma unroll
      for (int q = 0; q < 4; ++q) {
#pragma unroll
        for (int bl = 0; bl < 4; ++bl) {
          const float t1 = (float)(16 * gam + 4 * w4 + 64 * q + bl) * pf, c1 = cosr(t1), s1 = sinr(t1);
          B1r[bl][q] = pk(c1, s1); B1i[bl][q] = pk(-s1, c1);
          const float t4 = (float)(4 * gam + q) * (af + (float)bl * (1.f / 256.f)), c4 = cosr(t4), s4 = sinr(t4);
          A4r[bl][q] = pk(c4, -s4); A4i[bl][q] = pk(s4, c4);
        }
        const float a = (float)(4 * gam + q) * (16.f * pf), cs = cosr(a), sn = sinr(a);
        Fr[q] = pk(cs, sn); Fi[q] = pk(-sn, cs); Gr[q] = pk(cs, -sn); Gi[q] = pk(sn, cs);
      }
#pragma unroll
      for (int q = 0; q < 4; ++q) { keep(B1r[q]); keep(B1i[q]); keep(A4r[q]); keep(A4i[q]); }
      keep(Fr); keep(Fi); keep(Gr); keep(Gi);
    }
    PIN();
    f32x2 xv[2][5];
    {
      float s[2], q[2];
#pragma unroll
      for (int i = 0; i < 2; ++i) {
        xv[i][0] = up2(xa[i][0]); xv[i][1] = up2(xa[i][1]); xv[i][2] = up2(xb[i][0]); xv[i][3] = up2(xb[i][1]); xv[i][4] = up2(xd[i]);
        f32x2 a = xv[i][0], c = xv[i][0] * xv[i][0];
#pragma unroll
        for (int k = 1; k < 5; ++k) { a += xv[i][k]; c += xv[i][k] * xv[i][k]; }
        s[i] = a[0] + a[1]; q[i] = c[0] + c[1];
      }
      const float v = row_sum(fold16(fold32(s[0], s[1]), fold32(q[0], q[1]))) * (1.f / D);
      const float m0 = rdl(v, 0), m1 = rdl(v, 32);
      const float r0 = __builtin_amdgcn_rsqf(fmaxf(rdl(v, 16) - m0 * m0, 0.f) + EPS), r1 = __builtin_amdgcn_rsqf(fmaxf(rdl(v, 48) - m1 * m1, 0.f) + EPS);
      f32x2 sm[5], df[5];
#pragma unroll
      for (int k = 0; k < 5; ++k) { const f32x2 z0 = (xv[0][k] - m0) * r0, z1 = (xv[1][k] - m1) * r1; sm[k] = z0 + z1; df[k] = z0 - z1; }
      float* z = Lf + L_Z + w4 * 640;
      *(f32x4*)(z + 4 * l) = (f32x4){sm[0][0], sm[0][1], sm[1][0], sm[1][1]};
      *(f32x4*)(z + 256 + 4 * l) = (f32x4){sm[2][0], sm[2][1], sm[3][0], sm[3][1]};
      *(f32x2*)(z + 512 + 2 * l) = sm[4];
      *(f32x4*)(z + 2560 + 4 * l) = (f32x4){df[0][0], df[0][1], df[1][0], df[1][1]};
      *(f32x4*)(z + 2560 + 256 + 4 * l) = (f32x4){df[2][0], df[2][1], df[3][0], df[3][1]};
      *(f32x2*)(z + 2560 + 512 + 2 * l) = df[4];
    }
    if (tr == 0) *(u32x2*)(L + L_Q) = (u32x2){0u, 0u};
    STAMP(1);
    asm volatile("" : "+s"(tag)::"memory");
    PRIO(2);
    BAR();
    const u32 tlo = (u32)(tag + 1), thi = (u32)((tag + 1) >> 32);
    {
      const int h = l >> 5, lp = l & 31;
      const float* z = Lf + L_Z + h * 2560 + 160 * w4;
      const float e = h ? 0.f : 1.f, kk = h ? 0.70710678f : 0.f, ek = e + kk, sg = h ? -1.f : 1.f, ne = 1.f - e;
      f32x4 z4[4]; float z1[4];
#pragma unroll
      for (int i = 0; i < 4; ++i) { z4[i] = *(const f32x4*)(z + i * 640 + 4 * lp); z1[i] = z[i * 640 + 128 + lp]; }
      f32x2 p0[5], p1[5];
#pragma unroll
      for (int k = 0; k < 5; ++k) {
        const float a = k < 4 ? z4[0][k] : z1[0], c = k < 4 ? z4[1][k] : z1[1], bb = k < 4 ? z4[2][k] : z1[2], d = k < 4 ? z4[3][k] : z1[3];
        const float sm = c + d, df = c - d;
        p0[k] = (f32x2){a + e * (bb + sm) + kk * df, e * a + sg * bb - ek * sm};
        p1[k] = (f32x2){a - e * bb - kk * df, ne * bb - kk * sm - e * df};
        keep(p0[k]); keep(p1[k]);
      }
      if (SLEEP_M) __builtin_amdgcn_s_sleep(SLEEP_M);
      const u32 mo = O_MOD + xc * MOD_REP + 640 * w4;
      const u32 o4 = mo + 16 * lp, o1 = mo + 512 + 8 * (lp >> 1), want = (lp & 2) ? thi : tlo;
      const u32 hm = h ? 0u : ~0u;
      u32* u = L + L_U + h * 640 + 5 * w4;
      u32* ua = u + (lp & 7) * 20 + (lp >> 3);
      u32* ub = u + ((lp >> 2) + 8 * (lp & 3)) * 20 + 4;
      u32x4 c4, s4; u32x2 c1, s1;
      STAMP(2);
#pragma nounroll
      for (int sp = 0; sp < SPIN; ++sp) {
        REREAD();
        c4 = ld16(rs, o4 + 2560); c1 = ld8(rs, o1 + 2560);
        s4 = ld16(rs, h ? NONE : o4); s1 = ld8(rs, h ? NONE : o1);
        const u32 bad = (c4[1] ^ tlo) | (c4[3] ^ thi) | (c1[1] ^ want) | (((s4[1] ^ tlo) | (s4[3] ^ thi) | (s1[1] ^ want)) & hm);
        if (!__any(bad != 0)) break;
        RETRY(0x100);
      }
      STAMP(3);
      const float A[5] = {lo16(c4[0]), hi16(c4[0]), lo16(c4[2]), hi16(c4[2]), (lp & 1) ? hi16(c1[0]) : lo16(c1[0])};
      const float B[5] = {lo16(s4[0]), hi16(s4[0]), lo16(s4[2]), hi16(s4[2]), (lp & 1) ? hi16(s1[0]) : lo16(s1[0])};
#pragma unroll
      for (int k = 0; k < 5; ++k) {
        const f32x2 q1 = p1[k] * A[k];
        u32* o = k < 4 ? ua + k * 160 : ub;
        o[0] = pk(p0[k][0] * A[k] + B[k], p0[k][1] * A[k]);
        o[1280] = pk(q1[0], q1[1]);
      }
    }
    BAR();
    PRIO(0);
    STAMP(4);
    if (w4 < 3) warm((u64)wmod, 640 * 7680, tr < 160 && (tr & 7) < 5 ? (20 * j + (tr >> 3)) * 7680 + 2560 + 640 * xc + 128 * (tr & 7) : NONE, ldsd);
    if (w4 == 3 && col) {
      __builtin_amdgcn_s_sleep(SLEEP_T);
#pragma nounroll
      for (int sp = 0; sp < SPIN; ++sp) {
        REREAD();
        const u32x4 g = ld16(rs, O_TOK + xc * TOK_REP + l * 16), e = ld16(rs, O_TOK + xc * TOK_REP + 1024 + l * 16);
        if (!__any(((g[0] ^ tlo) | (g[1] ^ thi) | (g[2] ^ tlo) | (g[3] ^ thi) | (e[0] ^ tlo) | (e[1] ^ thi) | (e[2] ^ tlo) | (e[3] ^ thi)) != 0)) break;
        __builtin_amdgcn_s_sleep(8);
        RETRY(0x10000);
      }
    }
    STAMP(5);
    BAR();
    STAMP(6);
    if (col) {
      PRIO(2);
      const u32 o = O_T + seg * SEGB + w4 * 4096 + l * 4;
      u32x4 in[4];
#pragma unroll
      for (int i = 0; i < 16; ++i) in[i >> 2][i & 3] = ld4(rs, o + i * 256);
      PIN();
      f32x4 fr[4], fi[4];
      {
        const bool hw = herm && w4 == 0;
        const float h0 = ((k1 & 3) == 0 && kap0 == 0 ? 0.5f : 1.f) * (hw ? 0.5f : 1.f), h2 = (k1 & 3) == 0 && kap0 == 8 ? 0.5f : 1.f;
        u32x4 v[4]; u32 e[4];
#pragma unroll
        for (int r = 0; r < 4; ++r) {
          v[r] = *(const u32x4*)(L + L_W + (m16 + 16 * (4 * gam + r)) * 16 + w4 * 4);
          e[r] = L[L_W2 + m16 + 16 * (4 * gam + r)] & (hw ? ~0u : 0u);
        }
#pragma unroll
        for (int r = 0; r < 4; ++r) {
#pragma unroll
          for (int c = 0; c < 4; ++c) { fr[c][r] = lo16(v[r][c]); fi[c][r] = hi16(v[r][c]); }
          fr[0][r] = (fr[0][r] + lo16(e[r])) * h0; fi[0][r] = (fi[0][r] + hi16(e[r])) * h0;
          fr[2][r] *= h2; fi[2][r] *= h2;
        }
      }
#pragma unroll
      for (int c = 0; c < 4; ++c) { keep(fr[c]); keep(fi[c]); }
      PIN();
      const f32x4 z = {0, 0, 0, 0};
      f32x4 yr[4], yi[4];
#pragma nounroll
      for (int sp = 0; sp < SPIN; ++sp) {
#pragma unroll
        for (int bl = 0; bl < 4; ++bl) { yr[bl] = mfma16(in[bl], B1r[bl], z); yi[bl] = mfma16(in[bl], B1i[bl], z); }
        if (!__any(max(max(umax4(in[0]), umax4(in[1])), max(umax4(in[2]), umax4(in[3]))) == SENT)) break;
        REREAD();
#pragma unroll
        for (int i = 0; i < 16; ++i) in[i >> 2][i & 3] = ld4(rs, o + i * 256);
        RETRY(1);
      }
      STAMP(7);
      u32* x1w = L + L_X1 + gam * 1024 + w4 * 64 + m16 * 4;
#pragma unroll
      for (int q = 0; q < 4; ++q)
        *(u32x4*)(x1w + q * 256) = (u32x4){pk(yr[0][q], yi[0][q]), pk(yr[1][q], yi[1][q]), pk(yr[2][q], yi[2][q]), pk(yr[3][q], yi[3][q])};
      BAR();
      u32x4 a[4];
#pragma unroll
      for (int c = 0; c < 4; ++c) a[c] = *(const u32x4*)(L + L_X1 + c * 1024 + w4 * 256 + l * 4);
#pragma unroll
      for (int c = 0; c < 4; ++c) { yr[c] = mfma16(Fr, a[c], z); yi[c] = mfma16(Fi, a[c], z); }
#pragma unroll
      for (int c = 0; c < 4; ++c) {
        u32 o0, o1, o2, o3;
        cmul2(pr(yr[c], 0), pr(yi[c], 0), pr(fr[c], 0), pr(fi[c], 0), o0, o1);
        cmul2(pr(yr[c], 2), pr(yi[c], 2), pr(fr[c], 2), pr(fi[c], 2), o2, o3);
        a[c] = (u32x4){o0, o1, o2, o3};
        yr[c] = mfma16(a[c], Gr, z); yi[c] = mfma16(a[c], Gi, z);
      }
      u32* x2w = L + L_X2 + m16 * 256 + gam * 64;
#pragma unroll
      for (int c = 0; c < 4; ++c)
        *(u32x4*)(x2w + (((4 * c + w4) ^ m16) << 2)) =
            (u32x4){pk(yr[c][0], yi[c][0]), pk(yr[c][1], yi[c][1]), pk(yr[c][2], yi[c][2]), pk(yr[c][3], yi[c][3])};
      BAR();
#pragma unroll
      for (int bl = 0; bl < 4; ++bl) a[bl] = *(const u32x4*)(L + L_X2 + (4 * w4 + bl) * 256 + gam * 64 + ((m16 ^ (4 * w4 + bl)) << 2));
#pragma unroll
      for (int bl = 0; bl < 4; ++bl) { yr[bl] = mfma16(A4r[bl], a[bl], z); yi[bl] = mfma16(A4i[bl], a[bl], z); }
#pragma unroll
      for (int i = 0; i < 16; ++i) st4(rs, o + i * 256, ~(pk(yi[i >> 2][i & 3], yr[i >> 2][i & 3]) ^ in[i >> 2][i & 3]));
      PRIO(0);
      STAMP(8);
      DRAIN();
      STAMP(9);
      BAR();
    } else {
      BAR();
      __builtin_amdgcn_s_sleep(SLEEP_M2);
    }
    f32x2 ga[5], cb[5], sb[5], gb[5], vv[5];
    float sv, sbw;
    {
      const u32 mo = O_MOD + xc * MOD_REP + 5120 + w4 * 2560 + l * 16;
      const u32x2 wra = *(const u32x2*)(wrt + 4 * l), wrb = *(const u32x2*)(wrt + 256 + 4 * l);
      const u32 wrd = *(const u32*)(wrt + 512 + 2 * l);
      u32x4 g0, g1, g2;
#pragma nounroll
      for (int sp = 0; sp < SPIN; ++sp) {
        REREAD();
        g0 = ld16(rs, mo); g1 = ld16(rs, mo + 1024); g2 = ld16(rs, l < 32 ? mo + 2048 : NONE);
        const u32 bad = (g0[1] ^ tlo) | (g0[3] ^ thi) | (g1[1] ^ tlo) | (g1[3] ^ thi) | (l < 32 ? (g2[1] ^ tlo) | (g2[3] ^ thi) : 0u);
        if (!__any(bad != 0)) break;
        __builtin_amdgcn_s_sleep(4);
        RETRY(0x10000);
      }
      u32* lm = L + L_M + w4 * 320 + 2 * l;
      *(u32x2*)lm = (u32x2){g0[0], g0[2]}; *(u32x2*)(lm + 128) = (u32x2){g1[0], g1[2]};
      if (l < 32) *(u32x2*)(lm + 256) = (u32x2){g2[0], g2[2]};
      BAR();
      auto ld5 = [&](const u32* p, f32x2 (&v)[5]) {
        const u32x2 a = *(const u32x2*)(p + 2 * l), c = *(const u32x2*)(p + 128 + 2 * l);
        v[0] = up2(a[0]); v[1] = up2(a[1]); v[2] = up2(c[0]); v[3] = up2(c[1]); v[4] = up2(p[256 + l]);
      };
      ld5(L + L_M, ga); ld5(L + L_M + 320, sb); ld5(L + L_M + 640, cb); ld5(L + L_M + 960, gb);
      vv[0] = up2(wra[0]); vv[1] = up2(wra[1]); vv[2] = up2(wrb[0]); vv[3] = up2(wrb[1]); vv[4] = up2(wrd);
      f32x2 a = {0, 0}, c = {0, 0};
#pragma unroll
      for (int k = 0; k < 5; ++k) { c += sb[k] * vv[k]; vv[k] *= cb[k]; a += vv[k]; ga[k] *= 1.f / ((float)S * (float)D); }
      const float v = fold32(a[0] + a[1], c[0] + c[1]), v2 = row_sum(fold16(v, v));
      sv = rdl(v2, 0); sbw = rdl(v2, 32);
    }
    u32 cf[4];
    cf[0] = pk(2.f, (w4 & 1) ? -2.f : 2.f);
#pragma unroll
    for (int g = 1; g < 4; ++g) { const float th = (float)(w4 * g) * 0.125f; cf[g] = pk(2.f * cosr(th), -2.f * sinr(th)); }
#pragma unroll
    for (int k = 0; k < 5; ++k) { keep(ga[k]); keep(cb[k]); keep(sb[k]); keep(gb[k]); keep(vv[k]); keep(xv[0][k]); keep(xv[1][k]); }
#pragma unroll
    for (int g = 0; g < 4; ++g) keep(cf[g]);
    u32 krow = w4 + 4 * (l >> 5);
    keep(krow); keep(sv); keep(sbw);
    if (!col) {
      if (j < 6) {
        const u32 o = (160 * w4 + (l >> 2)) * (F * 2) + (xc * 6 + j) * 64 + (l & 3) * 16;
#pragma nounroll
        for (int i = 0; i < 10; ++i) {
          glds16(wg, o + i * (16 * F * 2), lds0 + (L_T + w4 * 2560 + i * 256) * 4);
          glds16(wu, o + i * (16 * F * 2), lds0 + (L_T + 10240 + w4 * 2560 + i * 256) * 4);
        }
      } else {
        const u32 o = (384 * w4 + (l >> 1)) * (D * 2) + (xc * 5 + j - 6) * 32 + (l & 1) * 16;
        int k = 3 * (j & 3);
#pragma nounroll
        for (int i = 0; i < 12; ++i) { glds16(wd, o + k * (32 * D * 2), lds0 + (L_T + w4 * 3072 + k * 256) * 4); k = k == 11 ? 0 : k + 1; }
      }
    }
    STAMP(10);
    PRIO(2);
    BAR();
    STAMP(11);
    u32x4 ea[4], eb[4]; u32x2 ec[4];
#pragma unroll
    for (int g = 0; g < 4; ++g) {
      const u32* e = L + L_U + g * 640;
      ea[g] = *(const u32x4*)(e + 4 * l); eb[g] = *(const u32x4*)(e + 256 + 4 * l); ec[g] = *(const u32x2*)(e + 512 + 2 * l);
    }
    PIN();
    f32x2 sa_[2] = {}, qa[2] = {}, da[2] = {};
#pragma unroll
    for (int k = 0; k < 5; ++k) {
      f32x2 ev, od;
#pragma unroll
      for (int i = 0; i < 2; ++i) {
        auto el = [&](int g) { return k < 2 ? ea[g][2 * k + i] : (k < 4 ? eb[g][2 * k - 4 + i] : ec[g][i]); };
        ev[i] = dot2(el(2), cf[2], dot2(el(0), cf[0], 0.f));
        od[i] = dot2(el(3), cf[3], dot2(el(1), cf[1], 0.f));
      }
      xv[0][k] += ga[k] * (ev + od); xv[1][k] += ga[k] * (ev - od);
#pragma unroll
      for (int i = 0; i < 2; ++i) { sa_[i] += xv[i][k]; qa[i] += xv[i][k] * xv[i][k]; da[i] += xv[i][k] * vv[k]; }
    }
    const float dsum = fold32(da[0][0] + da[0][1], da[1][0] + da[1][1]);
    const float sq = row_sum(fold16(fold32(sa_[0][0] + sa_[0][1], sa_[1][0] + sa_[1][1]), fold32(qa[0][0] + qa[0][1], qa[1][0] + qa[1][1]))) * (1.f / D);
    const float dt = row_sum(fold16(dsum, dsum));
    const auto sw = __builtin_amdgcn_permlane16_swap(asu(sq), asu(sq), false, false);
    const float mean = asf(sw[0]), rstd = __builtin_amdgcn_rsqf(fmaxf(asf(sw[1]) - mean * mean, 0.f) + EPS);
    const u32 rb = asu(rstd * (dt - mean * sv) + sbw);
    const u32 key = (min(rb ^ ((u32)((int)rb >> 31) | 0x80000000u), 0xFFFFFFF0u) & ~7u) | krow;
    if ((l & 31) == 0) __hip_atomic_fetch_max(L + L_Q, key, __ATOMIC_RELAXED, __HIP_MEMORY_SCOPE_WORKGROUP);
    BAR();
    PRIO(0);
    if (!col) {
      DRAIN();
      BAR();
    }
    STAMP(12);
    const int best = __builtin_amdgcn_readfirstlane(L[L_Q]) & 7;
    const rsrc_t ro = __builtin_amdgcn_make_buffer_rsrc(out, 0, S * D * 2, 0x00020000);
#pragma unroll
    for (int i = 0; i < 2; ++i) {
      const float m = rdl(mean, 32 * i), rr = rdl(rstd, 32 * i);
      u32 hp[5], op[5];
#pragma unroll
      for (int k = 0; k < 5; ++k) {
        const f32x2 h2 = (xv[i][k] - m) * rr * cb[k] + sb[k], o = xv[i][k] + gb[k] * h2;
        hp[k] = pk(h2[0], h2[1]); op[k] = pk(o[0], o[1]);
      }
      if (best == w4 + 4 * i) {
        const u32 ho = O_H2 + pos * 1280;
        st8(rs, ho + 8 * l, (u32x2){hp[0], hp[1]}); st8(rs, ho + 512 + 8 * l, (u32x2){hp[2], hp[3]}); st4(rs, ho + 1024 + 4 * l, hp[4]);
      }
      const u32 oo = (s2 + 256 * w4 + 1024 * i) * 1280;
      st8(ro, oo + 8 * l, (u32x2){op[0], op[1]}); st8(ro, oo + 512 + 8 * l, (u32x2){op[2], op[3]}); st4(ro, oo + 1024 + 4 * l, op[4]);
    }
    if ((best & 3) == w4) {
      DRAIN();
      if (l == 0) st4(rs, O_DONE + pos * 16, 1u);
    }
    if (b == 255 && tr == 0) st8(rs, O_EPOCH, (u32x2){tlo, thi});
    STAMP(13);
    STAMP_OUT();
    return;
  }

  const int g = w, c = l & 31, h = l >> 5;
  const int nsg = g == 0 ? 6 : 5;
  const bool act = c < 4 * nsg;
  const u32 slot = act ? O_T + ((g == 0 ? 0 : 8 + 40 * g) + h * nsg + (c >> 2)) * SEGB + pos * 64 + (c & 3) * 16 : NONE;
  const u32 sstep = act ? 2 * nsg * SEGB : 0;
  const f32x4 z4 = {0, 0, 0, 0};
  u32x4 old[4], ar[4], ai[4], cr[3], ci[3];
  f32x2 uc[6], us[6];
  u32 tlo, thi;
  auto publish_mod = [&](const float* p, int col0) {
    const float s0 = p[l] + p[64 + l], s1 = fold32(s0, s0);
    float s = bfr(fold16(s1, s1));
    s = (col0 >= 640 && col0 < 1280) || (col0 >= 2560 && col0 < 3200) ? 1.f + s : (col0 < 640 ? 8.f * s : s);
    const float o = dppf<0xB1>(s);
    st8(rs, O_MOD + (gam + 4 * (l & 1)) * MOD_REP + col0 * 4 + (m16 >> 1) * 8, (u32x2){(l & 1) ? pk(o, s) : pk(s, o), (l & 2) ? thi : tlo});
  };
  {
    u32x2 wa[4], wc[4];
    const int kd = kap0 + 20 * (lam0 + (t & 3)), cw = mir ? D - 3 - kd : kd;
    auto ldw = [&](int i) {
      const int ks = k1 + 8 * (((t >> 2) + 64 * (i + seg)) & 255);
      const u32 o = ((mir ? (S - ks) & (S - 1) : ks) * DF + cw) * 2;
      wa[i] = *(const u32x2u*)((const char*)fwr + o); wc[i] = *(const u32x2u*)((const char*)fwi + o);
    };
    if (col) {
#pragma unroll
      for (int i = 0; i < WFIRST; ++i) ldw(i);
    }
    PIN();
    const float cf = (float)c;
#pragma unroll
    for (int tau = 0; tau < 3; ++tau) {
      const bool up = g == 0 && (tau == 2 || (tau == 1 && h));
      float kb = (float)(8 * tau + 4 * h - (up ? 12 : 0)), t1 = (float)(s2 * (g ? g : (up ? 4 : 0))) * (1.f / 2048.f);
      u32 mr = up ? 0u : ~0u, si = g ? 0x07060504u : (up ? 0x03020100u : 0x0c0c0c0cu);
      keep(kb); keep(t1); keep(mr); keep(si);
#pragma unroll
      for (int q = 0; q < 4; ++q) {
        const float kq = kb + (float)q;
        const float th = kq * (cf * (1.f / 640.f)) + t1;
        uc[2 * tau + (q >> 1)][q & 1] = cosr(th); us[2 * tau + (q >> 1)][q & 1] = sinr(th);
        const float t2 = kq * (cf * 0.05f), c2 = cosr(t2), s2_ = sinr(t2);
        const u32 p = pk(c2, -s2_);
        cr[tau][q] = p & mr;
        ci[tau][q] = __builtin_amdgcn_perm(pk(s2_, c2), p, si);
      }
    }
#pragma unroll
    for (int i = 0; i < 3; ++i) { keep(cr[i]); keep(ci[i]); }
#pragma unroll
    for (int i = 0; i < 6; ++i) { keep(uc[i]); keep(us[i]); }
    PIN();
    if (hasmod) {
      asm volatile("s_waitcnt vmcnt(5)" : "+v"(cf4)::"memory");
#pragma unroll
      for (int q = 0; q < 4; ++q) { const float a = lo16(cf4[q]), e = hi16(cf4[q]); cf4[q] = pk(a * sigm(a), e * sigm(e)); }
      keep(cf4);
    }
    {
      const u32x4 sent = {SENT, SENT, SENT, SENT};
      const u32 o = O_T + (t >> 2) * SEGB + pos * 64 + (t & 3) * 16;
      st16(rs, o, sent);
      st16(rs, o + 64 * SEGB, sent);
      const u32 oh = O_H2 + pos * 1280 + (t - 160) * 16, oc = (t < 248 ? O_CAND + (t - 240) * CAND_REP : O_DONE) + pos * 16;
      st16(rs, t < 160 ? o + 128 * SEGB : (t < 240 ? oh : oc), sent);
      st16(rs, t < 32 && j < 6 ? O_ACT + (t >> 2) * ACT_REP + (xc * 6 + j) * 64 + (t & 3) * 16 : NONE, sent);
    }
    PIN();
    u32 hr, hi;
    if (col) {
#pragma unroll
      for (int i = WFIRST; i < 4; ++i) ldw(i);
      const int ks = k1 + 8 * t;
      const u32 o2 = herm ? ((mir ? ks : (S - ks) & (S - 1)) * DF + 20 * lam0) * 2 : NONE;
      hr = (u32)__builtin_amdgcn_raw_buffer_load_b16(__builtin_amdgcn_make_buffer_rsrc((void*)fwr, 0, S * DF * 2, 0x00020000), o2, 0, 0);
      hi = (u32)__builtin_amdgcn_raw_buffer_load_b16(__builtin_amdgcn_make_buffer_rsrc((void*)fwi, 0, S * DF * 2, 0x00020000), o2, 0, 0);
    }
    PIN();
    STAMP(1);
    if (hasmod) {
      asm volatile("s_waitcnt vmcnt(4)" ::: "memory");
      STAMP(2);
      const LAS u32* p = (const LAS u32*)(L + L_G) + g * 2560 + gam * 64 + (m16 >> 2) * 8 + (m16 & 3) * 2;
      f32x4 d[5];
#pragma unroll
      for (int i = 0; i < 5; ++i) d[i] = mfma16(cf4, trfrag(p + i * 256, 32), z4);
      if (l < 32) Lf[L_Q + 16 + g * 32 + l] = gam ? d[4][0] : (d[0][0] + d[1][1]) + (d[2][2] + d[3][3]);
    }
    asm volatile("" : "+s"(tag)::"memory");
    tlo = (u32)(tag + 1); thi = (u32)((tag + 1) >> 32);
    BAR();
    if (w == 0 && hasmod) publish_mod(Lf + L_Q + 16, mb * 16);
    STAMP(3);
    const float kapf = (float)((g == 0 && c >= 12) ? c - 12 : c);
    const float k1f = g ? (float)g : (c >= 12 ? 4.f : 0.f);
    u32 sel = g ? 0x03020100u : (c < 11 ? 0x0c0c0100u : (c >= 12 && c < 23 ? 0x01000c0cu : 0x0c0c0c0cu)), sel2 = h ? 0x0c0c0c0cu : sel;
    keep(sel); keep(sel2);
    u32x4 br[3], bi[3];
    f32x2 tc[8], ts[8];
#pragma unroll
    for (int tau = 0; tau < 3; ++tau)
#pragma unroll
      for (int q = 0; q < 4; ++q) {
        const float th = (float)(8 * tau + 4 * h + q) * (kapf * 0.05f);
        const float cs = cosr(th), sn = sinr(th);
        br[tau][q] = __builtin_amdgcn_perm(0u, pk(cs, sn), tau == 2 ? sel2 : sel);
        bi[tau][q] = __builtin_amdgcn_perm(0u, pk(-sn, cs), tau == 2 ? sel2 : sel);
      }
    const float t0 = (float)s2 * k1f * (-1.f / 2048.f);
#pragma unroll
    for (int tau = 0; tau < 4; ++tau)
#pragma unroll
      for (int q = 0; q < 4; ++q) {
        const float n2 = (float)(8 * tau + 4 * h + q);
        const float th = n2 * (kapf * (-1.f / 640.f)) + t0;
        tc[2 * tau + (q >> 1)][q & 1] = cosr(th); ts[2 * tau + (q >> 1)][q & 1] = sinr(th);
        const float t2 = n2 * ((float)c * (1.f / 32.f)), c2 = cosr(t2), s2_ = sinr(t2);
        ar[tau][q] = pk(c2, s2_); ai[tau][q] = pk(-s2_, c2);
      }
#pragma unroll
    for (int i = 0; i < 3; ++i) { keep(br[i]); keep(bi[i]); }
#pragma unroll
    for (int i = 0; i < 4; ++i) { keep(ar[i]); keep(ai[i]); }
#pragma unroll
    for (int i = 0; i < 8; ++i) { keep(tc[i]); keep(ts[i]); }
    if (col) {
#pragma unroll
      for (int i = 0; i < 4; ++i) {
        u32x4 v = {__builtin_amdgcn_perm(wc[i][0], wa[i][0], 0x05040100), __builtin_amdgcn_perm(wc[i][0], wa[i][0], 0x07060302),
                   __builtin_amdgcn_perm(wc[i][1], wa[i][1], 0x05040100), __builtin_amdgcn_perm(wc[i][1], wa[i][1], 0x07060302)};
        if (mir) v = (u32x4){v[3] ^ 0x80000000u, v[2] ^ 0x80000000u, v[1] ^ 0x80000000u, v[0] ^ 0x80000000u};
        *(u32x4*)(L + L_W + ((t + 256 * (i + seg)) & 1023) * 4) = v;
      }
      keep(hr); keep(hi);
      L[L_W2 + t] = __builtin_amdgcn_perm(hi, hr, 0x05040100) ^ (mir ? 0u : 0x80000000u);
    }
    DRAIN();
    if (__builtin_amdgcn_readfirstlane(l == 0 ? __hip_atomic_fetch_add(L + L_Q + 1, 1u, __ATOMIC_RELAXED, __HIP_MEMORY_SCOPE_WORKGROUP) : 0u) == 3u)
      st8(rs, l < 8 ? O_TOK + l * TOK_REP + b * 8 : NONE, (u32x2){tlo, thi});
    const u32* u = L + L_U + g * 640 + ((c >> 2) + 8 * (c & 3)) * 20;
    STAMP(6);
    PRIO(2);
    BAR();
    STAMP(4);
    f32x16 yr = {}, yi = {};
    u32x4 a[3];
#pragma unroll
    for (int tau = 0; tau < 3; ++tau) a[tau] = *(const u32x4*)(u + (tau == 2 ? 16 : 8 * tau + 4 * h));
#pragma unroll
    for (int tau = 0; tau < 3; ++tau) { yr = mfma32(a[tau], br[tau], yr); yi = mfma32(a[tau], bi[tau], yi); }
    f32x16 zr = {}, zi = {};
#pragma unroll
    for (int tau = 0; tau < 4; ++tau) {
      u32 o0, o1, o2, o3;
      cmul2(pr(yr, 4 * tau), pr(yi, 4 * tau), tc[2 * tau], ts[2 * tau], o0, o1);
      cmul2(pr(yr, 4 * tau + 2), pr(yi, 4 * tau + 2), tc[2 * tau + 1], ts[2 * tau + 1], o2, o3);
      const u32x4 v = {o0, o1, o2, o3};
      zr = mfma32(ar[tau], v, zr);
      zi = mfma32(ai[tau], v, zi);
      PIN();
    }
#pragma unroll
    for (int i = 0; i < 4; ++i) {
      old[i] = (u32x4){pk(zr[4 * i], zi[4 * i]), pk(zr[4 * i + 1], zi[4 * i + 1]), pk(zr[4 * i + 2], zi[4 * i + 2]), pk(zr[4 * i + 3], zi[4 * i + 3])};
      st16(rs, slot + i * sstep, old[i]);
      PIN();
    }
    PRIO(0);
    STAMP(5);
    if (hasmod) {
      const u32 o = (160 * g + (l >> 2)) * 7680 + 2560 + mb * 64 + (l & 3) * 16;
      int k = 5 * (mb & 1);
#pragma nounroll
      for (int i = 0; i < 10; ++i) { glds16(wmod, o + k * (16 * 7680), lds0 + (L_G + g * 2560 + k * 256) * 4); k = k == 9 ? 0 : k + 1; }
    }
    if (!act)
#pragma unroll
      for (int i = 0; i < 4; ++i) old[i] = (u32x4){SENT, SENT, SENT, SENT};
#pragma unroll
    for (int i = 0; i < 4; ++i) keep(old[i]);
    if (col) DRAIN();
    BAR();
  }
  {
    auto warm_mlp = [&]() {
      const int r = l / 3;
      if (w < 2) warm((u64)(w ? wu : wg), D * F * 2, l < 60 ? (20 * j + r) * (F * 2) + 384 * xc + 128 * (l - 3 * r) : NONE, ldsd);
      else warm((u64)wd, D * F * 2, l < 48 ? (48 * j + 24 * (w - 2) + (l >> 1)) * (D * 2) + 160 * xc + (l & 1) * 156 : NONE, ldsd);
    };
    STAMP(7);
    if (col) {
      BAR();
      warm_mlp();
      BAR();
      BAR();
    } else {
      DRAIN();
      if (hasmod) {
        const LAS u32* p = (const LAS u32*)(L + L_G) + g * 2560 + gam * 128 + (m16 >> 2) * 16 + (m16 & 3) * 2;
#pragma unroll
        for (int n = 0; n < 2; ++n) {
          f32x4 d[5];
#pragma unroll
          for (int i = 0; i < 5; ++i) d[i] = mfma16(cf4, trfrag(p + i * 512 + n * 8, 64), z4);
          if (l < 32) Lf[L_Q + 16 + n * 128 + g * 32 + l] = gam ? d[4][0] : (d[0][0] + d[1][1]) + (d[2][2] + d[3][3]);
        }
      }
      BAR();
      if (w < 2 && hasmod) publish_mod(Lf + L_Q + 16 + w * 128, 1280 + mb * 32 + w * 16);
      warm_mlp();
      if (SLEEP_NC) __builtin_amdgcn_s_sleep(SLEEP_NC);
      const u32 co = l == 0 ? slot + 3 * sstep : NONE, want = l == 0 ? old[3][0] : 1u;
#pragma nounroll
      for (int sp = 0; sp < SPIN; ++sp) {
        REREAD();
        if (!__any(ld4(rs, co) == want)) break;
        __builtin_amdgcn_s_sleep(2);
      }
    }
    STAMP(8);
    PRIO(2);
    u32x4 a[4];
#pragma unroll
    for (int i = 0; i < 4; ++i) a[i] = ld16(rs, slot + i * sstep);
    asm volatile("s_barrier" ::: "memory");
    f32x16 yr, yi;
#pragma nounroll
    for (int sp = 0; sp < SPIN; ++sp) {
      yr = (f32x16){}; yi = (f32x16){};
#pragma unroll
      for (int i = 0; i < 4; ++i) { a[i] = ~(a[i] ^ old[i]); yr = mfma32(a[i], ai[i], yr); yi = mfma32(a[i], ar[i], yi); }
      if (!__any(max(max(umax4(a[0]), umax4(a[1])), max(umax4(a[2]), umax4(a[3]))) == SENT)) break;
      REREAD();
#pragma unroll
      for (int i = 0; i < 4; ++i) a[i] = ld16(rs, slot + i * sstep);
      RETRY(1);
    }
    STAMP(9);
    f32x16 zr = {}, zi = {};
#pragma unroll
    for (int tau = 0; tau < 3; ++tau) {
      u32 o0, o1, o2, o3;
      cmul2(pr(yr, 4 * tau), pr(yi, 4 * tau), uc[2 * tau], us[2 * tau], o0, o1);
      cmul2(pr(yr, 4 * tau + 2), pr(yi, 4 * tau + 2), uc[2 * tau + 1], us[2 * tau + 1], o2, o3);
      const u32x4 v = {o0, o1, o2, o3};
      zr = mfma32(cr[tau], v, zr);
      zi = mfma32(ci[tau], v, zi);
      PIN();
    }
    u32* e = L + L_U + g * 640 + h * 128 + c;
#pragma unroll
    for (int r = 0; r < 8; ++r) e[(r >> 2) * 256 + (r & 3) * 32] = pk(zr[r], zi[r]);
    if (h == 0)
#pragma unroll
      for (int r = 8; r < 12; ++r) e[512 + (r & 3) * 32] = pk(zr[r], zi[r]);
    STAMP(10);
    BAR();
    PRIO(0);
  }
  const bool gu = j < 6;
  PRIO(3);
  BAR();
  STAMP(11);
  if (w == 0) st4(rs, l < 8 ? O_CAND + l * CAND_REP + pos * 16 : NONE, L[L_Q]);
  if (col) { STAMP_OUT(); return; }
  BAR();
  u32x4 wf[20];
  {
    if (gu) {
      const LAS u32* p = (const LAS u32*)(L + L_T) + (m16 & 2) * 5120 + gam * 128 + (m16 >> 2) * 16 + g * 4 + (m16 & 1) * 2;
#pragma unroll
      for (int i = 0; i < 20; ++i) wf[i] = trfrag(p + i * 512, 64);
    } else {
      const LAS u32* p = (const LAS u32*)(L + L_T) + g * 3072 + gam * 64 + (m16 >> 2) * 8 + (m16 & 3) * 2;
#pragma unroll
      for (int i = 0; i < 12; ++i) wf[i] = trfrag(p + i * 256, 32);
#pragma unroll
      for (int i = 12; i < 20; ++i) wf[i] = (u32x4){0, 0, 0, 0};
    }
  }
#pragma unroll
  for (int i = 0; i < 20; i += 4) asm volatile("" : "+v"(wf[i]), "+v"(wf[i + 1]), "+v"(wf[i + 2]), "+v"(wf[i + 3]));
  u32 kmax = 0;
  int win = 0;
  if (w == 0) {
    DRAIN();
    u32x4 ck;
    const u32 co = O_CAND + xc * CAND_REP + l * 16;
#pragma nounroll
    for (int sp = 0; sp < SPIN; ++sp) {
      REREAD();
#pragma unroll
      for (int i = 0; i < 4; ++i) ck[i] = ld4(rs, co + i * 1024);
      if (!__any(umax4(ck) == SENT)) break;
      RETRY(0x100);
    }
    const u32 mine = umax4(ck);
    const int wi_ = l + (ck[0] == mine ? 0 : (ck[1] == mine ? 64 : (ck[2] == mine ? 128 : 192)));
    u32 v = mine;
    v = max(v, dppu<0xB1>(v)); v = max(v, dppu<0x4E>(v)); v = max(v, dppu<0x141>(v)); v = max(v, dppu<0x140>(v));
    kmax = max(max(__builtin_amdgcn_readlane(v, 0), __builtin_amdgcn_readlane(v, 16)), max(__builtin_amdgcn_readlane(v, 32), __builtin_amdgcn_readlane(v, 48)));
    win = __builtin_amdgcn_readlane(wi_, __builtin_ctzll(__ballot(mine == kmax)));
    STAMP(12);
  }
  if (gu) {
    if (w == 0) {
      const u32 ho = O_H2 + win * 1280 + l * 16, ho2 = l < 16 ? ho + 1024 : NONE;
      u32x4 a, e;
#pragma nounroll
      for (int sp = 0; sp < SPIN; ++sp) {
        REREAD();
        a = ld16(rs, ho); e = ld16(rs, ho2);
        *(u32x4*)(L + L_H + l * 4) = a;
        if (l < 16) *(u32x4*)(L + L_H + 256 + l * 4) = e;
        if (!__any(max(umax4(a), umax4(e)) == SENT)) break;
        RETRY(0x10000);
      }
    }
    BAR();
    STAMP(13);
    f32x4 d = z4;
#pragma unroll
    for (int i = 0; i < 20; ++i) d = mfma16(*(const u32x4*)(L + L_H + i * 16 + gam * 4), wf[i], d);
    const float gg = bfr(d[0]), av = bfr(gg * sigm(gg)) * bfr(dppf<0x128>(d[0]));
    const float o = dppf<0xB1>(av);
    st4(rs, m16 < 8 ? O_ACT + (gam + 4 * (l & 1)) * ACT_REP + (xc * 6 + j) * 64 + g * 16 + (m16 >> 1) * 4 : NONE, (l & 1) ? pk(o, av) : pk(av, o));
  } else {
    const int dni = xc * 5 + j - 6;
    const rsrc_t ro = __builtin_amdgcn_make_buffer_rsrc(out, 0, S * D * 2, 0x00020000);
    const u32 oo = l < 8 ? (16 * (win & 15) + (win >> 4) + 256 * (kmax & 7)) * 1280 + dni * 32 + l * 4 : NONE;
    f32x2 base = {0, 0}, gs = {0, 0};
    if (w == 0) {
#pragma nounroll
      for (int sp = 0; sp < SPIN; ++sp) {
        REREAD();
        if (!__any(ld4(rs, O_DONE + win * 16) == SENT)) break;
      }
      REREAD();
      base = up2(ld4(ro, oo));
      const u32 rb = (kmax & 0x80000000u) ? (kmax & 0x7FFFFFFFu) : ~kmax;
      gs = up2(L[L_M + 960 + dni * 8 + (l & 7)]) * bfr(sigm(bfr(asf(rb))));
      keep(base); keep(gs);
    } else {
      __builtin_amdgcn_s_sleep(SLEEP_C);
    }
    const u32 ao = m16 < 12 ? O_ACT + xc * ACT_REP + (12 * g + m16) * 64 + gam * 16 : NONE;
    u32x4 a;
#pragma nounroll
    for (int sp = 0; sp < SPIN; ++sp) {
      REREAD();
      a = ld16(rs, ao);
      if (!__any(umax4(a) == SENT)) break;
      RETRY(0x10000);
    }
    STAMP(13);
    float s = 0;
#pragma unroll
    for (int r = 0; r < 4; ++r) {
      const f32x4 d0 = mfma16(a, wf[r], z4), d1 = mfma16(a, wf[4 + r], z4), d2 = mfma16(a, wf[8 + r], z4);
      s += gam == 0 ? d0[r] : (gam == 1 ? d1[r] : d2[r]);
    }
    s = fold32(s, s);
    s = fold16(s, s);
    if (l < 16) Lf[L_Q + 16 + g * 16 + l] = s;
    BAR();
    if (w == 0) {
      const float* p = Lf + L_Q + 16 + (l & 7) * 2;
      const f32x2 y = (*(const f32x2*)p + *(const f32x2*)(p + 16)) + (*(const f32x2*)(p + 32) + *(const f32x2*)(p + 48));
      const f32x2 o = base + gs * y;
      st4(ro, oo, pk(o[0], o[1]));
    }
  }
  STAMP(14);
  STAMP_OUT();
}

extern "C" void kernel_launch(void* const* d_in, const int* in_sizes, int n_in, void* d_out, int out_size, void* d_ws, size_t ws_size,
                              hipStream_t stream) {
  fused<<<256, 512, 0, stream>>>((const u16*)d_in[0], (const u16*)d_in[8], (const u16*)d_in[9], (char*)d_ws, (const u16*)d_in[1], (const u16*)d_in[2],
                                 (const u16*)d_in[4], (const u16*)d_in[5], (const u16*)d_in[6], (const u16*)d_in[7], (u16*)d_out);
}
```

```cpp
#include <hip/hip_runtime.h>
#include <stdint.h>

typedef unsigned u32;
typedef unsigned short u16;
typedef unsigned long long u64;
typedef u32 u32x2 __attribute__((ext_vector_type(2)));
typedef u32 u32x4 __attribute__((ext_vector_type(4)));
typedef u32 u32x2u __attribute__((ext_vector_type(2), aligned(2)));
typedef float f32x2 __attribute__((ext_vector_type(2)));
typedef float f32x4 __attribute__((ext_vector_type(4)));
typedef float f32x16 __attribute__((ext_vector_type(16)));
typedef short s16x4 __attribute__((ext_vector_type(4)));
typedef __bf16 bf16x2 __attribute__((ext_vector_type(2)));
typedef __bf16 bf16x8 __attribute__((ext_vector_type(8)));
typedef __amdgpu_buffer_rsrc_t rsrc_t;
#define DI __device__ __forceinline__
#define LAS __attribute__((address_space(3)))
#define GAS __attribute__((address_space(1)))
#define NWAVES 8

constexpr int S = 2048, D = 640, DF = 321, F = 1536;
constexpr float EPS = 1e-5f;
constexpr u32 SENT = 0xFFFFFFFFu, NONE = 0xFFFFFF00u;
constexpr int SPIN = 1 << 16;

constexpr u32 O_EPOCH = 0;
constexpr u32 O_TOK = 212992, TOK_REP = 2048;
constexpr u32 O_DONE = 8192;
constexpr u32 O_CAND = 16384, CAND_REP = 4096;
constexpr u32 O_ACT = 49152, ACT_REP = 4096;
constexpr u32 O_MOD = 81920, MOD_REP = 16384;
constexpr u32 O_H2 = 262144;
constexpr u32 SEGB = 16384 + 2304;
constexpr u32 O_T = 1u << 20;
constexpr u32 O_STAMP = 7u << 20;
constexpr u32 WS_USED = 8u << 20;

constexpr int L_U = 0;
constexpr int L_M = 2560;
constexpr int L_Q = 3840;
constexpr int L_H = 4352;
constexpr int L_G = 4672;
constexpr int L_T = 14912;
constexpr int L_Z = L_T;
constexpr int L_W = L_G;
constexpr int L_X1 = L_G + 4096;
constexpr int L_W2 = L_G + 8192;
constexpr int L_X2 = L_T + 5120;
constexpr int L_D = 35392;
constexpr int L_END = 35456;

DI u32 pk(float lo, float hi) { return __builtin_bit_cast(u32, __builtin_convertvector((f32x2){lo, hi}, bf16x2)); }
DI float asf(u32 v) { return __builtin_bit_cast(float, v); }
DI u32 asu(float v) { return __builtin_bit_cast(u32, v); }
DI float lo16(u32 v) { return asf(v << 16); }
DI float hi16(u32 v) { return asf(v & 0xffff0000u); }
DI f32x2 up2(u32 v) { return (f32x2){lo16(v), hi16(v)}; }
DI float bfr(float x) { return lo16(pk(x, 0.f)); }
DI float ldbf(const u16* p) { return lo16(*p); }
DI f32x16 mfma32(u32x4 a, u32x4 b, f32x16 c) {
  return __builtin_amdgcn_mfma_f32_32x32x16_bf16(__builtin_bit_cast(bf16x8, a), __builtin_bit_cast(bf16x8, b), c, 0, 0, 0);
}
DI f32x4 mfma16(u32x4 a, u32x4 b, f32x4 c) {
  return __builtin_amdgcn_mfma_f32_16x16x32_bf16(__builtin_bit_cast(bf16x8, a), __builtin_bit_cast(bf16x8, b), c, 0, 0, 0);
}
DI float dot2(u32 a, u32 b, float c) {
  return __builtin_amdgcn_fdot2_f32_bf16(__builtin_bit_cast(bf16x2, a), __builtin_bit_cast(bf16x2, b), c, false);
}
DI float cosr(float rev) { return __builtin_amdgcn_cosf(rev); }
DI float sinr(float rev) { return __builtin_amdgcn_sinf(rev); }
template <int CTRL> DI u32 dppu(u32 v) { return (u32)__builtin_amdgcn_update_dpp(0, (int)v, CTRL, 0xf, 0xf, true); }
template <int CTRL> DI float dppf(float v) { return asf(dppu<CTRL>(asu(v))); }
DI float row_sum(float v) {
  v += dppf<0xB1>(v); v += dppf<0x4E>(v); v += dppf<0x141>(v); v += dppf<0x140>(v);
  return v;
}
DI float fold32(float a, float b) { auto r = __builtin_amdgcn_permlane32_swap(asu(a), asu(b), false, false); return asf(r[0]) + asf(r[1]); }
DI float fold16(float a, float b) { auto r = __builtin_amdgcn_permlane16_swap(asu(a), asu(b), false, false); return asf(r[0]) + asf(r[1]); }
DI float rdl(float v, int lane) { return asf(__builtin_amdgcn_readlane(asu(v), lane)); }
DI float sigm(float x) { return __builtin_amdgcn_rcpf(1.f + __expf(-x)); }
DI u32 umax4(u32x4 v) { return max(max(v[0], v[1]), max(v[2], v[3])); }
template <class V> DI f32x2 pr(const V& v, int i) { return (f32x2){v[i], v[i + 1]}; }
DI void cmul2(f32x2 r, f32x2 im, f32x2 c, f32x2 s, u32& o0, u32& o1) {
  const f32x2 a = r * c - im * s, b = im * c + r * s;
  o0 = pk(a[0], b[0]); o1 = pk(a[1], b[1]);
}
DI u32x4 trfrag(const LAS u32* p, int hi) {
  const u32x2 a = __builtin_bit_cast(u32x2, __builtin_amdgcn_ds_read_tr16_b64_v4i16((LAS s16x4*)p));
  const u32x2 b = __builtin_bit_cast(u32x2, __builtin_amdgcn_ds_read_tr16_b64_v4i16((LAS s16x4*)(p + hi)));
  return (u32x4){a[0], a[1], b[0], b[1]};
}

DI u32x4 ld16(rsrc_t r, u32 o) { return __builtin_amdgcn_raw_buffer_load_b128(r, o, 0, 16); }
DI u32x2 ld8(rsrc_t r, u32 o) { return __builtin_amdgcn_raw_buffer_load_b64(r, o, 0, 16); }
DI u32 ld4(rsrc_t r, u32 o) { return __builtin_amdgcn_raw_buffer_load_b32(r, o, 0, 16); }
DI void st16(rsrc_t r, u32 o, u32x4 v) { __builtin_amdgcn_raw_buffer_store_b128(v, r, o, 0, 16); }
DI void st8(rsrc_t r, u32 o, u32x2 v) { __builtin_amdgcn_raw_buffer_store_b64(v, r, o, 0, 16); }
DI void st4(rsrc_t r, u32 o, u32 v) { __builtin_amdgcn_raw_buffer_store_b32(v, r, o, 0, 16); }
#define DRAIN() asm volatile("s_waitcnt vmcnt(0)" ::: "memory")
#define BAR() do { __builtin_amdgcn_sched_barrier(0); asm volatile("s_waitcnt lgkmcnt(0)\n\ts_barrier" ::: "memory"); __builtin_amdgcn_sched_barrier(0); } while (0)
#define REREAD() asm volatile("" ::: "memory")
#define PIN() __builtin_amdgcn_sched_barrier(0)
#define PRIO(n) __builtin_amdgcn_s_setprio(n)
template <class T> DI void keep(T& v) { asm volatile("" : "+v"(v)); }
DI void glds16(const void* base, u32 off, u32 lds_byte) {
  u32 m;
  asm volatile("s_mov_b32 %0, m0\n\ts_mov_b32 m0, %3\n\ts_nop 0\n\tglobal_load_lds_dwordx4 %1, %2\n\ts_mov_b32 m0, %0"
               : "=&s"(m) : "v"(off), "s"(base), "s"(lds_byte) : "memory");
}
DI void warm(u64 base, u32 bytes, u32 off, u32 lds_byte) {
  const u32x4 d = {(u32)base, (u32)(base >> 32) & 0xffffu, bytes, 0x00020000u};
  u32 m;
  asm volatile("s_mov_b32 %0, m0\n\ts_mov_b32 m0, %3\n\ts_nop 0\n\tbuffer_load_dword %1, %2, 0 offen lds\n\ts_mov_b32 m0, %0"
               : "=&s"(m) : "v"(off), "s"(d), "s"(lds_byte) : "memory");
}
#define STAMP(i) do { if (i == 0 || i == 3) { u64 t_; PIN(); asm volatile("s_memrealtime %0\n\ts_waitcnt lgkmcnt(0)" : "=s"(t_)::"memory"); PIN(); tdiag = (u32)t_ - tdiag; } } while (0)
#define STAMP_OUT() do { if (blockIdx.x == 48 && w == 0) { const u64 t0_ = __builtin_amdgcn_s_memrealtime(); \
    while ((u32)(__builtin_amdgcn_s_memrealtime() - t0_) < min(tdiag, 5000u)) {} } } while (0)
#define RETRY(v)

#define WFIRST 1
#define SLEEP_M 0
#define SLEEP_T 12
#define SLEEP_M2 16
#define SLEEP_NC 0
#define SLEEP_C 40

__global__ __launch_bounds__(512, 2) void fused(const u16* __restrict__ x, const u16* __restrict__ cvec, const u16* __restrict__ wmod, char* ws,
                                                const u16* __restrict__ fwr, const u16* __restrict__ fwi, const u16* __restrict__ wrt,
                                                const u16* __restrict__ wg, const u16* __restrict__ wu, const u16* __restrict__ wd, u16* out) {
  __shared__ __attribute__((aligned(16))) u32 L[L_END];
  u32 tdiag = 0;
  STAMP(0);
  u64 code_ptr;
  u32 code_len;
  asm volatile("s_getpc_b64 %0\n.Lcode_from:\n\ts_mov_b32 %1, .Lcode_to-.Lcode_from-4\n\t.subsection 1\n.Lcode_to:\n\t.subsection 0"
               : "=s"(code_ptr), "=s"(code_len));
  const u32 lds0 = (u32)(size_t)(LAS u32*)L, ldsd = lds0 + L_D * 4;
  warm(code_ptr, code_len, (threadIdx.x * 4 + ((blockIdx.x >> 3) & 3)) * 128u, ldsd);
  asm volatile("" ::"s"(x), "s"(cvec), "s"(wmod), "s"(ws), "s"(fwr), "s"(fwi), "s"(wrt), "s"(wg), "s"(wu), "s"(wd), "s"(out));
  const int b = blockIdx.x, xc = b & 7, j = b >> 3, t = threadIdx.x, l = t & 63;
  const int w = __builtin_amdgcn_readfirstlane(t >> 6), w4 = w & 3;
  const int pos = xc * 32 + j, s2 = 16 * (pos & 15) + (pos >> 4);
  const bool col = j >= 11, hasmod = j < 10;
  const int seg = xc * 21 + j - 11, mb = xc * 10 + j;
  const int m16 = l & 15, gam = l >> 4;
  const rsrc_t rs = __builtin_amdgcn_make_buffer_rsrc(ws, 0, WS_USED, 0x00020000);
  float* Lf = (float*)L;

  u32x2 xa[2], xb[2];
  u32 xd[2];
  u32x4 cf4;
  u64 tag = *(const __attribute__((address_space(4))) u64*)(size_t)(ws + O_EPOCH);
  if (w >= 4) {
#pragma unroll
    for (int i = 0; i < 2; ++i) {
      const u16* xrow = x + (size_t)(s2 + 256 * w4 + 1024 * i) * D;
      xa[i] = *(const u32x2*)(xrow + 4 * l); xb[i] = *(const u32x2*)(xrow + 256 + 4 * l); xd[i] = *(const u32*)(xrow + 512 + 2 * l);
    }
  }
  if (w < 4 && hasmod) {
    asm volatile("global_load_dwordx4 %0, %1, %2" : "=v"(cf4) : "v"(320 * w4 + 64 * min(m16, 4) + 16 * gam), "s"(cvec) : "memory");
    const u32 o = (160 * w4 + (l >> 1)) * 7680 + mb * 32 + (l & 1) * 16;
    int k = mb & 3;
#pragma unroll
    for (int i = 0; i < 5; ++i) { glds16(wmod, o + k * (32 * 7680), lds0 + (L_G + w4 * 2560 + k * 256) * 4); k = k == 4 ? 0 : k + 1; }
  }
  if (w < 2 && col) warm((u64)wmod, 640 * 7680, (t & 3) < 3 ? (32 * (j - 11) + (t >> 2)) * 7680 + 320 * xc + (t & 3) * 156 : NONE, ldsd);
  PIN();
  int k1 = 0, kap0 = 0, lam0 = 0;
  if (col) {
    if (seg < 48) { const int c0 = (seg % 6) * 4; k1 = c0 >= 12 ? 4 : 0; kap0 = c0 >= 12 ? c0 - 12 : c0; lam0 = 4 * (seg / 6); }
    else { k1 = 1 + (seg - 48) / 40; kap0 = ((seg - 48) % 5) * 4; lam0 = 4 * (((seg - 48) % 40) / 5); }
  }
  const bool mir = lam0 >= 16, herm = col && kap0 == 0 && (lam0 & 15) == 0;

  if (w >= 4) {
    const int tr = t - 256;
    u32x4 B1r[4], B1i[4], A4r[4], A4i[4], Fr, Fi, Gr, Gi;
    if (col) {
      const float pf = (float)m16 * (1.f / 256.f), af = (float)(16 * ((m16 >> 2) + 4 * (m16 & 3)) + 4 * w4) * (1.f / 256.f);
#pragma unroll
      for (int q = 0; q < 4; ++q) {
#pragma unroll
        for (int bl = 0; bl < 4; ++bl) {
          const float t1 = (float)(16 * gam + 4 * w4 + 64 * q + bl) * pf, c1 = cosr(t1), s1 = sinr(t1);
          B1r[bl][q] = pk(c1, s1); B1i[bl][q] = pk(-s1, c1);
          const float t4 = (float)(4 * gam + q) * (af + (float)bl * (1.f / 256.f)), c4 = cosr(t4), s4 = sinr(t4);
          A4r[bl][q] = pk(c4, -s4); A4i[bl][q] = pk(s4, c4);
        }
        const float a = (float)(4 * gam + q) * (16.f * pf), cs = cosr(a), sn = sinr(a);
        Fr[q] = pk(cs, sn); Fi[q] = pk(-sn, cs); Gr[q] = pk(cs, -sn); Gi[q] = pk(sn, cs);
      }
#pragma unroll
      for (int q = 0; q < 4; ++q) { keep(B1r[q]); keep(B1i[q]); keep(A4r[q]); keep(A4i[q]); }
      keep(Fr); keep(Fi); keep(Gr); keep(Gi);
    }
    PIN();
    f32x2 xv[2][5];
    {
      float s[2], q[2];
#pragma unroll
      for (int i = 0; i < 2; ++i) {
        xv[i][0] = up2(xa[i][0]); xv[i][1] = up2(xa[i][1]); xv[i][2] = up2(xb[i][0]); xv[i][3] = up2(xb[i][1]); xv[i][4] = up2(xd[i]);
        f32x2 a = xv[i][0], c = xv[i][0] * xv[i][0];
#pragma unroll
        for (int k = 1; k < 5; ++k) { a += xv[i][k]; c += xv[i][k] * xv[i][k]; }
        s[i] = a[0] + a[1]; q[i] = c[0] + c[1];
      }
      const float v = row_sum(fold16(fold32(s[0], s[1]), fold32(q[0], q[1]))) * (1.f / D);
      const float m0 = rdl(v, 0), m1 = rdl(v, 32);
      const float r0 = __builtin_amdgcn_rsqf(fmaxf(rdl(v, 16) - m0 * m0, 0.f) + EPS), r1 = __builtin_amdgcn_rsqf(fmaxf(rdl(v, 48) - m1 * m1, 0.f) + EPS);
      f32x2 sm[5], df[5];
#pragma unroll
      for (int k = 0; k < 5; ++k) { const f32x2 z0 = (xv[0][k] - m0) * r0, z1 = (xv[1][k] - m1) * r1; sm[k] = z0 + z1; df[k] = z0 - z1; }
      float* z = Lf + L_Z + w4 * 640;
      *(f32x4*)(z + 4 * l) = (f32x4){sm[0][0], sm[0][1], sm[1][0], sm[1][1]};
      *(f32x4*)(z + 256 + 4 * l) = (f32x4){sm[2][0], sm[2][1], sm[3][0], sm[3][1]};
      *(f32x2*)(z + 512 + 2 * l) = sm[4];
      *(f32x4*)(z + 2560 + 4 * l) = (f32x4){df[0][0], df[0][1], df[1][0], df[1][1]};
      *(f32x4*)(z + 2560 + 256 + 4 * l) = (f32x4){df[2][0], df[2][1], df[3][0], df[3][1]};
      *(f32x2*)(z + 2560 + 512 + 2 * l) = df[4];
    }
    if (tr == 0) *(u32x2*)(L + L_Q) = (u32x2){0u, 0u};
    STAMP(1);
    asm volatile("" : "+s"(tag)::"memory");
    PRIO(2);
    BAR();
    const u32 tlo = (u32)(tag + 1), thi = (u32)((tag + 1) >> 32);
    {
      const int h = l >> 5, lp = l & 31;
      const float* z = Lf + L_Z + h * 2560 + 160 * w4;
      const float e = h ? 0.f : 1.f, kk = h ? 0.70710678f : 0.f, ek = e + kk, sg = h ? -1.f : 1.f, ne = 1.f - e;
      f32x4 z4[4]; float z1[4];
#pragma unroll
      for (int i = 0; i < 4; ++i) { z4[i] = *(const f32x4*)(z + i * 640 + 4 * lp); z1[i] = z[i * 640 + 128 + lp]; }
      f32x2 p0[5], p1[5];
#pragma unroll
      for (int k = 0; k < 5; ++k) {
        const float a = k < 4 ? z4[0][k] : z1[0], c = k < 4 ? z4[1][k] : z1[1], bb = k < 4 ? z4[2][k] : z1[2], d = k < 4 ? z4[3][k] : z1[3];
        const float sm = c + d, df = c - d;
        p0[k] = (f32x2){a + e * (bb + sm) + kk * df, e * a + sg * bb - ek * sm};
        p1[k] = (f32x2){a - e * bb - kk * df, ne * bb - kk * sm - e * df};
        keep(p0[k]); keep(p1[k]);
      }
      if (SLEEP_M) __builtin_amdgcn_s_sleep(SLEEP_M);
      const u32 mo = O_MOD + xc * MOD_REP + 640 * w4;
      const u32 o4 = mo + 16 * lp, o1 = mo + 512 + 8 * (lp >> 1), want = (lp & 2) ? thi : tlo;
      const u32 hm = h ? 0u : ~0u;
      u32* u = L + L_U + h * 640 + 5 * w4;
      u32* ua = u + (lp & 7) * 20 + (lp >> 3);
      u32* ub = u + ((lp >> 2) + 8 * (lp & 3)) * 20 + 4;
      u32x4 c4, s4; u32x2 c1, s1;
      STAMP(2);
#pragma nounroll
      for (int sp = 0; sp < SPIN; ++sp) {
        REREAD();
        c4 = ld16(rs, o4 + 2560); c1 = ld8(rs, o1 + 2560);
        s4 = ld16(rs, h ? NONE : o4); s1 = ld8(rs, h ? NONE : o1);
        const u32 bad = (c4[1] ^ tlo) | (c4[3] ^ thi) | (c1[1] ^ want) | (((s4[1] ^ tlo) | (s4[3] ^ thi) | (s1[1] ^ want)) & hm);
        if (!__any(bad != 0)) break;
        RETRY(0x100);
      }
      STAMP(3);
      const float A[5] = {lo16(c4[0]), hi16(c4[0]), lo16(c4[2]), hi16(c4[2]), (lp & 1) ? hi16(c1[0]) : lo16(c1[0])};
      const float B[5] = {lo16(s4[0]), hi16(s4[0]), lo16(s4[2]), hi16(s4[2]), (lp & 1) ? hi16(s1[0]) : lo16(s1[0])};
#pragma unroll
      for (int k = 0; k < 5; ++k) {
        const f32x2 q1 = p1[k] * A[k];
        u32* o = k < 4 ? ua + k * 160 : ub;
        o[0] = pk(p0[k][0] * A[k] + B[k], p0[k][1] * A[k]);
        o[1280] = pk(q1[0], q1[1]);
      }
    }
    BAR();
    PRIO(0);
    STAMP(4);
    if (w4 < 3) warm((u64)wmod, 640 * 7680, tr < 160 && (tr & 7) < 5 ? (20 * j + (tr >> 3)) * 7680 + 2560 + 640 * xc + 128 * (tr & 7) : NONE, ldsd);
    if (w4 == 3 && col) {
      __builtin_amdgcn_s_sleep(SLEEP_T);
#pragma nounroll
      for (int sp = 0; sp < SPIN; ++sp) {
        REREAD();
        const u32x4 g = ld16(rs, O_TOK + xc * TOK_REP + l * 16), e = ld16(rs, O_TOK + xc * TOK_REP + 1024 + l * 16);
        if (!__any(((g[0] ^ tlo) | (g[1] ^ thi) | (g[2] ^ tlo) | (g[3] ^ thi) | (e[0] ^ tlo) | (e[1] ^ thi) | (e[2] ^ tlo) | (e[3] ^ thi)) != 0)) break;
        __builtin_amdgcn_s_sleep(8);
        RETRY(0x10000);
      }
    }
    STAMP(5);
    BAR();
    STAMP(6);
    if (col) {
      PRIO(2);
      const u32 o = O_T + seg * SEGB + w4 * 4096 + l * 4;
      u32x4 in[4];
#pragma unroll
      for (int i = 0; i < 16; ++i) in[i >> 2][i & 3] = ld4(rs, o + i * 256);
      PIN();
      f32x4 fr[4], fi[4];
      {
        const bool hw = herm && w4 == 0;
        const float h0 = ((k1 & 3) == 0 && kap0 == 0 ? 0.5f : 1.f) * (hw ? 0.5f : 1.f), h2 = (k1 & 3) == 0 && kap0 == 8 ? 0.5f : 1.f;
        u32x4 v[4]; u32 e[4];
#pragma unroll
        for (int r = 0; r < 4; ++r) {
          v[r] = *(const u32x4*)(L + L_W + (m16 + 16 * (4 * gam + r)) * 16 + w4 * 4);
          e[r] = L[L_W2 + m16 + 16 * (4 * gam + r)] & (hw ? ~0u : 0u);
        }
#pragma unroll
        for (int r = 0; r < 4; ++r) {
#pragma unroll
          for (int c = 0; c < 4; ++c) { fr[c][r] = lo16(v[r][c]); fi[c][r] = hi16(v[r][c]); }
          fr[0][r] = (fr[0][r] + lo16(e[r])) * h0; fi[0][r] = (fi[0][r] + hi16(e[r])) * h0;
          fr[2][r] *= h2; fi[2][r] *= h2;
        }
      }
#pragma unroll
      for (int c = 0; c < 4; ++c) { keep(fr[c]); keep(fi[c]); }
      PIN();
      const f32x4 z = {0, 0, 0, 0};
      f32x4 yr[4], yi[4];
#pragma nounroll
      for (int sp = 0; sp < SPIN; ++sp) {
#pragma unroll
        for (int bl = 0; bl < 4; ++bl) { yr[bl] = mfma16(in[bl], B1r[bl], z); yi[bl] = mfma16(in[bl], B1i[bl], z); }
        if (!__any(max(max(umax4(in[0]), umax4(in[1])), max(umax4(in[2]), umax4(in[3]))) == SENT)) break;
        REREAD();
#pragma unroll
        for (int i = 0; i < 16; ++i) in[i >> 2][i & 3] = ld4(rs, o + i * 256);
        RETRY(1);
      }
      STAMP(7);
      u32* x1w = L + L_X1 + gam * 1024 + w4 * 64 + m16 * 4;
#pragma unroll
      for (int q = 0; q < 4; ++q)
        *(u32x4*)(x1w + q * 256) = (u32x4){pk(yr[0][q], yi[0][q]), pk(yr[1][q], yi[1][q]), pk(yr[2][q], yi[2][q]), pk(yr[3][q], yi[3][q])};
      BAR();
      u32x4 a[4];
#pragma unroll
      for (int c = 0; c < 4; ++c) a[c] = *(const u32x4*)(L + L_X1 + c * 1024 + w4 * 256 + l * 4);
#pragma unroll
      for (int c = 0; c < 4; ++c) { yr[c] = mfma16(Fr, a[c], z); yi[c] = mfma16(Fi, a[c], z); }
#pragma unroll
      for (int c = 0; c < 4; ++c) {
        u32 o0, o1, o2, o3;
        cmul2(pr(yr[c], 0), pr(yi[c], 0), pr(fr[c], 0), pr(fi[c], 0), o0, o1);
        cmul2(pr(yr[c], 2), pr(yi[c], 2), pr(fr[c], 2), pr(fi[c], 2), o2, o3);
        a[c] = (u32x4){o0, o1, o2, o3};
        yr[c] = mfma16(a[c], Gr, z); yi[c] = mfma16(a[c], Gi, z);
      }
      u32* x2w = L + L_X2 + m16 * 256 + gam * 64;
#pragma unroll
      for (int c = 0; c < 4; ++c)
        *(u32x4*)(x2w + (((4 * c + w4) ^ m16) << 2)) =
            (u32x4){pk(yr[c][0], yi[c][0]), pk(yr[c][1], yi[c][1]), pk(yr[c][2], yi[c][2]), pk(yr[c][3], yi[c][3])};
      BAR();
#pragma unroll
      for (int bl = 0; bl < 4; ++bl) a[bl] = *(const u32x4*)(L + L_X2 + (4 * w4 + bl) * 256 + gam * 64 + ((m16 ^ (4 * w4 + bl)) << 2));
#pragma unroll
      for (int bl = 0; bl < 4; ++bl) { yr[bl] = mfma16(A4r[bl], a[bl], z); yi[bl] = mfma16(A4i[bl], a[bl], z); }
#pragma unroll
      for (int i = 0; i < 16; ++i) st4(rs, o + i * 256, ~(pk(yi[i >> 2][i & 3], yr[i >> 2][i & 3]) ^ in[i >> 2][i & 3]));
      PRIO(0);
      STAMP(8);
      DRAIN();
      STAMP(9);
      BAR();
    } else {
      BAR();
      __builtin_amdgcn_s_sleep(SLEEP_M2);
    }
    f32x2 ga[5], cb[5], sb[5], gb[5], vv[5];
    float sv, sbw;
    {
      const u32 mo = O_MOD + xc * MOD_REP + 5120 + w4 * 2560 + l * 16;
      const u32x2 wra = *(const u32x2*)(wrt + 4 * l), wrb = *(const u32x2*)(wrt + 256 + 4 * l);
      const u32 wrd = *(const u32*)(wrt + 512 + 2 * l);
      u32x4 g0, g1, g2;
#pragma nounroll
      for (int sp = 0; sp < SPIN; ++sp) {
        REREAD();
        g0 = ld16(rs, mo); g1 = ld16(rs, mo + 1024); g2 = ld16(rs, l < 32 ? mo + 2048 : NONE);
        const u32 bad = (g0[1] ^ tlo) | (g0[3] ^ thi) | (g1[1] ^ tlo) | (g1[3] ^ thi) | (l < 32 ? (g2[1] ^ tlo) | (g2[3] ^ thi) : 0u);
        if (!__any(bad != 0)) break;
        __builtin_amdgcn_s_sleep(4);
        RETRY(0x10000);
      }
      u32* lm = L + L_M + w4 * 320 + 2 * l;
      *(u32x2*)lm = (u32x2){g0[0], g0[2]}; *(u32x2*)(lm + 128) = (u32x2){g1[0], g1[2]};
      if (l < 32) *(u32x2*)(lm + 256) = (u32x2){g2[0], g2[2]};
      BAR();
      auto ld5 = [&](const u32* p, f32x2 (&v)[5]) {
        const u32x2 a = *(const u32x2*)(p + 2 * l), c = *(const u32x2*)(p + 128 + 2 * l);
        v[0] = up2(a[0]); v[1] = up2(a[1]); v[2] = up2(c[0]); v[3] = up2(c[1]); v[4] = up2(p[256 + l]);
      };
      ld5(L + L_M, ga); ld5(L + L_M + 320, sb); ld5(L + L_M + 640, cb); ld5(L + L_M + 960, gb);
      vv[0] = up2(wra[0]); vv[1] = up2(wra[1]); vv[2] = up2(wrb[0]); vv[3] = up2(wrb[1]); vv[4] = up2(wrd);
      f32x2 a = {0, 0}, c = {0, 0};
#pragma unroll
      for (int k = 0; k < 5; ++k) { c += sb[k] * vv[k]; vv[k] *= cb[k]; a += vv[k]; ga[k] *= 1.f / ((float)S * (float)D); }
      const float v = fold32(a[0] + a[1], c[0] + c[1]), v2 = row_sum(fold16(v, v));
      sv = rdl(v2, 0); sbw = rdl(v2, 32);
    }
    u32 cf[4];
    cf[0] = pk(2.f, (w4 & 1) ? -2.f : 2.f);
#pragma unroll
    for (int g = 1; g < 4; ++g) { const float th = (float)(w4 * g) * 0.125f; cf[g] = pk(2.f * cosr(th), -2.f * sinr(th)); }
#pragma unroll
    for (int k = 0; k < 5; ++k) { keep(ga[k]); keep(cb[k]); keep(sb[k]); keep(gb[k]); keep(vv[k]); keep(xv[0][k]); keep(xv[1][k]); }
#pragma unroll
    for (int g = 0; g < 4; ++g) keep(cf[g]);
    u32 krow = w4 + 4 * (l >> 5);
    keep(krow); keep(sv); keep(sbw);
    if (!col) {
      if (j < 6) {
        const u32 o = (160 * w4 + (l >> 2)) * (F * 2) + (xc * 6 + j) * 64 + (l & 3) * 16;
#pragma nounroll
        for (int i = 0; i < 10; ++i) {
          glds16(wg, o + i * (16 * F * 2), lds0 + (L_T + w4 * 2560 + i * 256) * 4);
          glds16(wu, o + i * (16 * F * 2), lds0 + (L_T + 10240 + w4 * 2560 + i * 256) * 4);
        }
      } else {
        const u32 o = (384 * w4 + (l >> 1)) * (D * 2) + (xc * 5 + j - 6) * 32 + (l & 1) * 16;
        int k = 3 * (j & 3);
#pragma nounroll
        for (int i = 0; i < 12; ++i) { glds16(wd, o + k * (32 * D * 2), lds0 + (L_T + w4 * 3072 + k * 256) * 4); k = k == 11 ? 0 : k + 1; }
      }
    }
    STAMP(10);
    PRIO(2);
    BAR();
    STAMP(11);
    u32x4 ea[4], eb[4]; u32x2 ec[4];
#pragma unroll
    for (int g = 0; g < 4; ++g) {
      const u32* e = L + L_U + g * 640;
      ea[g] = *(const u32x4*)(e + 4 * l); eb[g] = *(const u32x4*)(e + 256 + 4 * l); ec[g] = *(const u32x2*)(e + 512 + 2 * l);
    }
    PIN();
    f32x2 sa_[2] = {}, qa[2] = {}, da[2] = {};
#pragma unroll
    for (int k = 0; k < 5; ++k) {
      f32x2 ev, od;
#pragma unroll
      for (int i = 0; i < 2; ++i) {
        auto el = [&](int g) { return k < 2 ? ea[g][2 * k + i] : (k < 4 ? eb[g][2 * k - 4 + i] : ec[g][i]); };
        ev[i] = dot2(el(2), cf[2], dot2(el(0), cf[0], 0.f));
        od[i] = dot2(el(3), cf[3], dot2(el(1), cf[1], 0.f));
      }
      xv[0][k] += ga[k] * (ev + od); xv[1][k] += ga[k] * (ev - od);
#pragma unroll
      for (int i = 0; i < 2; ++i) { sa_[i] += xv[i][k]; qa[i] += xv[i][k] * xv[i][k]; da[i] += xv[i][k] * vv[k]; }
    }
    const float dsum = fold32(da[0][0] + da[0][1], da[1][0] + da[1][1]);
    const float sq = row_sum(fold16(fold32(sa_[0][0] + sa_[0][1], sa_[1][0] + sa_[1][1]), fold32(qa[0][0] + qa[0][1], qa[1][0] + qa[1][1]))) * (1.f / D);
    const float dt = row_sum(fold16(dsum, dsum));
    const auto sw = __builtin_amdgcn_permlane16_swap(asu(sq), asu(sq), false, false);
    const float mean = asf(sw[0]), rstd = __builtin_amdgcn_rsqf(fmaxf(asf(sw[1]) - mean * mean, 0.f) + EPS);
    const u32 rb = asu(rstd * (dt - mean * sv) + sbw);
    const u32 key = (min(rb ^ ((u32)((int)rb >> 31) | 0x80000000u), 0xFFFFFFF0u) & ~7u) | krow;
    if ((l & 31) == 0) __hip_atomic_fetch_max(L + L_Q, key, __ATOMIC_RELAXED, __HIP_MEMORY_SCOPE_WORKGROUP);
    BAR();
    PRIO(0);
    if (!col) {
      DRAIN();
      BAR();
    }
    STAMP(12);
    const int best = __builtin_amdgcn_readfirstlane(L[L_Q]) & 7;
    const rsrc_t ro = __builtin_amdgcn_make_buffer_rsrc(out, 0, S * D * 2, 0x00020000);
#pragma unroll
    for (int i = 0; i < 2; ++i) {
      const float m = rdl(mean, 32 * i), rr = rdl(rstd, 32 * i);
      u32 hp[5], op[5];
#pragma unroll
      for (int k = 0; k < 5; ++k) {
        const f32x2 h2 = (xv[i][k] - m) * rr * cb[k] + sb[k], o = xv[i][k] + gb[k] * h2;
        hp[k] = pk(h2[0], h2[1]); op[k] = pk(o[0], o[1]);
      }
      if (best == w4 + 4 * i) {
        const u32 ho = O_H2 + pos * 1280;
        st8(rs, ho + 8 * l, (u32x2){hp[0], hp[1]}); st8(rs, ho + 512 + 8 * l, (u32x2){hp[2], hp[3]}); st4(rs, ho + 1024 + 4 * l, hp[4]);
      }
      const u32 oo = (s2 + 256 * w4 + 1024 * i) * 1280;
      st8(ro, oo + 8 * l, (u32x2){op[0], op[1]}); st8(ro, oo + 512 + 8 * l, (u32x2){op[2], op[3]}); st4(ro, oo + 1024 + 4 * l, op[4]);
    }
    if ((best & 3) == w4) {
      DRAIN();
      if (l == 0) st4(rs, O_DONE + pos * 16, 1u);
    }
    if (b == 255 && tr == 0) st8(rs, O_EPOCH, (u32x2){tlo, thi});
    STAMP(13);
    STAMP_OUT();
    return;
  }

  const int g = w, c = l & 31, h = l >> 5;
  const int nsg = g == 0 ? 6 : 5;
  const bool act = c < 4 * nsg;
  const u32 slot = act ? O_T + ((g == 0 ? 0 : 8 + 40 * g) + h * nsg + (c >> 2)) * SEGB + pos * 64 + (c & 3) * 16 : NONE;
  const u32 sstep = act ? 2 * nsg * SEGB : 0;
  const f32x4 z4 = {0, 0, 0, 0};
  u32x4 old[4], ar[4], ai[4], cr[3], ci[3];
  f32x2 uc[6], us[6];
  u32 tlo, thi;
  auto publish_mod = [&](const float* p, int col0) {
    const float s0 = p[l] + p[64 + l], s1 = fold32(s0, s0);
    float s = bfr(fold16(s1, s1));
    s = (col0 >= 640 && col0 < 1280) || (col0 >= 2560 && col0 < 3200) ? 1.f + s : (col0 < 640 ? 8.f * s : s);
    const float o = dppf<0xB1>(s);
    st8(rs, O_MOD + (gam + 4 * (l & 1)) * MOD_REP + col0 * 4 + (m16 >> 1) * 8, (u32x2){(l & 1) ? pk(o, s) : pk(s, o), (l & 2) ? thi : tlo});
  };
  {
    u32x2 wa[4], wc[4];
    const int kd = kap0 + 20 * (lam0 + (t & 3)), cw = mir ? D - 3 - kd : kd;
    auto ldw = [&](int i) {
      const int ks = k1 + 8 * (((t >> 2) + 64 * (i + seg)) & 255);
      const u32 o = ((mir ? (S - ks) & (S - 1) : ks) * DF + cw) * 2;
      wa[i] = *(const u32x2u*)((const char*)fwr + o); wc[i] = *(const u32x2u*)((const char*)fwi + o);
    };
    if (col) {
#pragma unroll
      for (int i = 0; i < WFIRST; ++i) ldw(i);
    }
    PIN();
    const float cf = (float)c;
#pragma unroll
    for (int tau = 0; tau < 3; ++tau) {
      const bool up = g == 0 && (tau == 2 || (tau == 1 && h));
      float kb = (float)(8 * tau + 4 * h - (up ? 12 : 0)), t1 = (float)(s2 * (g ? g : (up ? 4 : 0))) * (1.f / 2048.f);
      u32 mr = up ? 0u : ~0u, si = g ? 0x07060504u : (up ? 0x03020100u : 0x0c0c0c0cu);
      keep(kb); keep(t1); keep(mr); keep(si);
#pragma unroll
      for (int q = 0; q < 4; ++q) {
        const float kq = kb + (float)q;
        const float th = kq * (cf * (1.f / 640.f)) + t1;
        uc[2 * tau + (q >> 1)][q & 1] = cosr(th); us[2 * tau + (q >> 1)][q & 1] = sinr(th);
        const float t2 = kq * (cf * 0.05f), c2 = cosr(t2), s2_ = sinr(t2);
        const u32 p = pk(c2, -s2_);
        cr[tau][q] = p & mr;
        ci[tau][q] = __builtin_amdgcn_perm(pk(s2_, c2), p, si);
      }
    }
#pragma unroll
    for (int i = 0; i < 3; ++i) { keep(cr[i]); keep(ci[i]); }
#pragma unroll
    for (int i = 0; i < 6; ++i) { keep(uc[i]); keep(us[i]); }
    PIN();
    if (hasmod) {
      asm volatile("s_waitcnt vmcnt(5)" : "+v"(cf4)::"memory");
#pragma unroll
      for (int q = 0; q < 4; ++q) { const float a = lo16(cf4[q]), e = hi16(cf4[q]); cf4[q] = pk(a * sigm(a), e * sigm(e)); }
      keep(cf4);
    }
    {
      const u32x4 sent = {SENT, SENT, SENT, SENT};
      const u32 o = O_T + (t >> 2) * SEGB + pos * 64 + (t & 3) * 16;
      st16(rs, o, sent);
      st16(rs, o + 64 * SEGB, sent);
      const u32 oh = O_H2 + pos * 1280 + (t - 160) * 16, oc = (t < 248 ? O_CAND + (t - 240) * CAND_REP : O_DONE) + pos * 16;
      st16(rs, t < 160 ? o + 128 * SEGB : (t < 240 ? oh : oc), sent);
      st16(rs, t < 32 && j < 6 ? O_ACT + (t >> 2) * ACT_REP + (xc * 6 + j) * 64 + (t & 3) * 16 : NONE, sent);
    }
    PIN();
    u32 hr, hi;
    if (col) {
#pragma unroll
      for (int i = WFIRST; i < 4; ++i) ldw(i);
      const int ks = k1 + 8 * t;
      const u32 o2 = herm ? ((mir ? ks : (S - ks) & (S - 1)) * DF + 20 * lam0) * 2 : NONE;
      hr = (u32)__builtin_amdgcn_raw_buffer_load_b16(__builtin_amdgcn_make_buffer_rsrc((void*)fwr, 0, S * DF * 2, 0x00020000), o2, 0, 0);
      hi = (u32)__builtin_amdgcn_raw_buffer_load_b16(__builtin_amdgcn_make_buffer_rsrc((void*)fwi, 0, S * DF * 2, 0x00020000), o2, 0, 0);
    }
    PIN();
    STAMP(1);
    if (hasmod) {
      asm volatile("s_waitcnt vmcnt(4)" ::: "memory");
      STAMP(2);
      const LAS u32* p = (const LAS u32*)(L + L_G) + g * 2560 + gam * 64 + (m16 >> 2) * 8 + (m16 & 3) * 2;
      f32x4 d[5];
#pragma unroll
      for (int i = 0; i < 5; ++i) d[i] = mfma16(cf4, trfrag(p + i * 256, 32), z4);
      if (l < 32) Lf[L_Q + 16 + g * 32 + l] = gam ? d[4][0] : (d[0][0] + d[1][1]) + (d[2][2] + d[3][3]);
    }
    asm volatile("" : "+s"(tag)::"memory");
    tlo = (u32)(tag + 1); thi = (u32)((tag + 1) >> 32);
    BAR();
    if (w == 0 && hasmod) publish_mod(Lf + L_Q + 16, mb * 16);
    STAMP(3);
    const float kapf = (float)((g == 0 && c >= 12) ? c - 12 : c);
    const float k1f = g ? (float)g : (c >= 12 ? 4.f : 0.f);
    u32 sel = g ? 0x03020100u : (c < 11 ? 0x0c0c0100u : (c >= 12 && c < 23 ? 0x01000c0cu : 0x0c0c0c0cu)), sel2 = h ? 0x0c0c0c0cu : sel;
    keep(sel); keep(sel2);
    u32x4 br[3], bi[3];
    f32x2 tc[8], ts[8];
#pragma unroll
    for (int tau = 0; tau < 3; ++tau)
#pragma unroll
      for (int q = 0; q < 4; ++q) {
        const float th = (float)(8 * tau + 4 * h + q) * (kapf * 0.05f);
        const float cs = cosr(th), sn = sinr(th);
        br[tau][q] = __builtin_amdgcn_perm(0u, pk(cs, sn), tau == 2 ? sel2 : sel);
        bi[tau][q] = __builtin_amdgcn_perm(0u, pk(-sn, cs), tau == 2 ? sel2 : sel);
      }
    const float t0 = (float)s2 * k1f * (-1.f / 2048.f);
#pragma unroll
    for (int tau = 0; tau < 4; ++tau)
#pragma unroll
      for (int q = 0; q < 4; ++q) {
        const float n2 = (float)(8 * tau + 4 * h + q);
        const float th = n2 * (kapf * (-1.f / 640.f)) + t0;
        tc[2 * tau + (q >> 1)][q & 1] = cosr(th); ts[2 * tau + (q >> 1)][q & 1] = sinr(th);
        const float t2 = n2 * ((float)c * (1.f / 32.f)), c2 = cosr(t2), s2_ = sinr(t2);
        ar[tau][q] = pk(c2, s2_); ai[tau][q] = pk(-s2_, c2);
      }
#pragma unroll
    for (int i = 0; i < 3; ++i) { keep(br[i]); keep(bi[i]); }
#pragma unroll
    for (int i = 0; i < 4; ++i) { keep(ar[i]); keep(ai[i]); }
#pragma unroll
    for (int i = 0; i < 8; ++i) { keep(tc[i]); keep(ts[i]); }
    if (col) {
#pragma unroll
      for (int i = 0; i < 4; ++i) {
        u32x4 v = {__builtin_amdgcn_perm(wc[i][0], wa[i][0], 0x05040100), __builtin_amdgcn_perm(wc[i][0], wa[i][0], 0x07060302),
                   __builtin_amdgcn_perm(wc[i][1], wa[i][1], 0x05040100), __builtin_amdgcn_perm(wc[i][1], wa[i][1], 0x07060302)};
        if (mir) v = (u32x4){v[3] ^ 0x80000000u, v[2] ^ 0x80000000u, v[1] ^ 0x80000000u, v[0] ^ 0x80000000u};
        *(u32x4*)(L + L_W + ((t + 256 * (i + seg)) & 1023) * 4) = v;
      }
      keep(hr); keep(hi);
      L[L_W2 + t] = __builtin_amdgcn_perm(hi, hr, 0x05040100) ^ (mir ? 0u : 0x80000000u);
    }
    DRAIN();
    if (__builtin_amdgcn_readfirstlane(l == 0 ? __hip_atomic_fetch_add(L + L_Q + 1, 1u, __ATOMIC_RELAXED, __HIP_MEMORY_SCOPE_WORKGROUP) : 0u) == 3u)
      st8(rs, l < 8 ? O_TOK + l * TOK_REP + b * 8 : NONE, (u32x2){tlo, thi});
    const u32* u = L + L_U + g * 640 + ((c >> 2) + 8 * (c & 3)) * 20;
    STAMP(6);
    PRIO(2);
    BAR();
    STAMP(4);
    f32x16 yr = {}, yi = {};
    u32x4 a[3];
#pragma unroll
    for (int tau = 0; tau < 3; ++tau) a[tau] = *(const u32x4*)(u + (tau == 2 ? 16 : 8 * tau + 4 * h));
#pragma unroll
    for (int tau = 0; tau < 3; ++tau) { yr = mfma32(a[tau], br[tau], yr); yi = mfma32(a[tau], bi[tau], yi); }
    f32x16 zr = {}, zi = {};
#pragma unroll
    for (int tau = 0; tau < 4; ++tau) {
      u32 o0, o1, o2, o3;
      cmul2(pr(yr, 4 * tau), pr(yi, 4 * tau), tc[2 * tau], ts[2 * tau], o0, o1);
      cmul2(pr(yr, 4 * tau + 2), pr(yi, 4 * tau + 2), tc[2 * tau + 1], ts[2 * tau + 1], o2, o3);
      const u32x4 v = {o0, o1, o2, o3};
      zr = mfma32(ar[tau], v, zr);
      zi = mfma32(ai[tau], v, zi);
      PIN();
    }
#pragma unroll
    for (int i = 0; i < 4; ++i) {
      old[i] = (u32x4){pk(zr[4 * i], zi[4 * i]), pk(zr[4 * i + 1], zi[4 * i + 1]), pk(zr[4 * i + 2], zi[4 * i + 2]), pk(zr[4 * i + 3], zi[4 * i + 3])};
      st16(rs, slot + i * sstep, old[i]);
      PIN();
    }
    PRIO(0);
    STAMP(5);
    if (hasmod) {
      const u32 o = (160 * g + (l >> 2)) * 7680 + 2560 + mb * 64 + (l & 3) * 16;
      int k = 5 * (mb & 1);
#pragma nounroll
      for (int i = 0; i < 10; ++i) { glds16(wmod, o + k * (16 * 7680), lds0 + (L_G + g * 2560 + k * 256) * 4); k = k == 9 ? 0 : k + 1; }
    }
    if (!act)
#pragma unroll
      for (int i = 0; i < 4; ++i) old[i] = (u32x4){SENT, SENT, SENT, SENT};
#pragma unroll
    for (int i = 0; i < 4; ++i) keep(old[i]);
    if (col) DRAIN();
    BAR();
  }
  {
    auto warm_mlp = [&]() {
      const int r = l / 3;
      if (w < 2) warm((u64)(w ? wu : wg), D * F * 2, l < 60 ? (20 * j + r) * (F * 2) + 384 * xc + 128 * (l - 3 * r) : NONE, ldsd);
      else warm((u64)wd, D * F * 2, l < 48 ? (48 * j + 24 * (w - 2) + (l >> 1)) * (D * 2) + 160 * xc + (l & 1) * 156 : NONE, ldsd);
    };
    STAMP(7);
    if (col) {
      BAR();
      warm_mlp();
      BAR();
      BAR();
    } else {
      DRAIN();
      if (hasmod) {
        const LAS u32* p = (const LAS u32*)(L + L_G) + g * 2560 + gam * 128 + (m16 >> 2) * 16 + (m16 & 3) * 2;
#pragma unroll
        for (int n = 0; n < 2; ++n) {
          f32x4 d[5];
#pragma unroll
          for (int i = 0; i < 5; ++i) d[i] = mfma16(cf4, trfrag(p + i * 512 + n * 8, 64), z4);
          if (l < 32) Lf[L_Q + 16 + n * 128 + g * 32 + l] = gam ? d[4][0] : (d[0][0] + d[1][1]) + (d[2][2] + d[3][3]);
        }
      }
      BAR();
      if (w < 2 && hasmod) publish_mod(Lf + L_Q + 16 + w * 128, 1280 + mb * 32 + w * 16);
      warm_mlp();
      if (SLEEP_NC) __builtin_amdgcn_s_sleep(SLEEP_NC);
      const u32 co = l == 0 ? slot + 3 * sstep : NONE, want = l == 0 ? old[3][0] : 1u;
#pragma nounroll
      for (int sp = 0; sp < SPIN; ++sp) {
        REREAD();
        if (!__any(ld4(rs, co) == want)) break;
        __builtin_amdgcn_s_sleep(2);
      }
    }
    STAMP(8);
    PRIO(2);
    u32x4 a[4];
#pragma unroll
    for (int i = 0; i < 4; ++i) a[i] = ld16(rs, slot + i * sstep);
    asm volatile("s_barrier" ::: "memory");
    f32x16 yr, yi;
#pragma nounroll
    for (int sp = 0; sp < SPIN; ++sp) {
      yr = (f32x16){}; yi = (f32x16){};
#pragma unroll
      for (int i = 0; i < 4; ++i) { a[i] = ~(a[i] ^ old[i]); yr = mfma32(a[i], ai[i], yr); yi = mfma32(a[i], ar[i], yi); }
      if (!__any(max(max(umax4(a[0]), umax4(a[1])), max(umax4(a[2]), umax4(a[3]))) == SENT)) break;
      REREAD();
#pragma unroll
      for (int i = 0; i < 4; ++i) a[i] = ld16(rs, slot + i * sstep);
      RETRY(1);
    }
    STAMP(9);
    f32x16 zr = {}, zi = {};
#pragma unroll
    for (int tau = 0; tau < 3; ++tau) {
      u32 o0, o1, o2, o3;
      cmul2(pr(yr, 4 * tau), pr(yi, 4 * tau), uc[2 * tau], us[2 * tau], o0, o1);
      cmul2(pr(yr, 4 * tau + 2), pr(yi, 4 * tau + 2), uc[2 * tau + 1], us[2 * tau + 1], o2, o3);
      const u32x4 v = {o0, o1, o2, o3};
      zr = mfma32(cr[tau], v, zr);
      zi = mfma32(ci[tau], v, zi);
      PIN();
    }
    u32* e = L + L_U + g * 640 + h * 128 + c;
#pragma unroll
    for (int r = 0; r < 8; ++r) e[(r >> 2) * 256 + (r & 3) * 32] = pk(zr[r], zi[r]);
    if (h == 0)
#pragma unroll
      for (int r = 8; r < 12; ++r) e[512 + (r & 3) * 32] = pk(zr[r], zi[r]);
    STAMP(10);
    BAR();
    PRIO(0);
  }
  const bool gu = j < 6;
  PRIO(3);
  BAR();
  STAMP(11);
  if (w == 0) st4(rs, l < 8 ? O_CAND + l * CAND_REP + pos * 16 : NONE, L[L_Q]);
  if (col) { STAMP_OUT(); return; }
  BAR();
  u32x4 wf[20];
  {
    if (gu) {
      const LAS u32* p = (const LAS u32*)(L + L_T) + (m16 & 2) * 5120 + gam * 128 + (m16 >> 2) * 16 + g * 4 + (m16 & 1) * 2;
#pragma unroll
      for (int i = 0; i < 20; ++i) wf[i] = trfrag(p + i * 512, 64);
    } else {
      const LAS u32* p = (const LAS u32*)(L + L_T) + g * 3072 + gam * 64 + (m16 >> 2) * 8 + (m16 & 3) * 2;
#pragma unroll
      for (int i = 0; i < 12; ++i) wf[i] = trfrag(p + i * 256, 32);
#pragma unroll
      for (int i = 12; i < 20; ++i) wf[i] = (u32x4){0, 0, 0, 0};
    }
  }
#pragma unroll
  for (int i = 0; i < 20; i += 4) asm volatile("" : "+v"(wf[i]), "+v"(wf[i + 1]), "+v"(wf[i + 2]), "+v"(wf[i + 3]));
  u32 kmax = 0;
  int win = 0;
  if (w == 0) {
    DRAIN();
    u32x4 ck;
    const u32 co = O_CAND + xc * CAND_REP + l * 16;
#pragma nounroll
    for (int sp = 0; sp < SPIN; ++sp) {
      REREAD();
#pragma unroll
      for (int i = 0; i < 4; ++i) ck[i] = ld4(rs, co + i * 1024);
      if (!__any(umax4(ck) == SENT)) break;
      RETRY(0x100);
    }
    const u32 mine = umax4(ck);
    const int wi_ = l + (ck[0] == mine ? 0 : (ck[1] == mine ? 64 : (ck[2] == mine ? 128 : 192)));
    u32 v = mine;
    v = max(v, dppu<0xB1>(v)); v = max(v, dppu<0x4E>(v)); v = max(v, dppu<0x141>(v)); v = max(v, dppu<0x140>(v));
    kmax = max(max(__builtin_amdgcn_readlane(v, 0), __builtin_amdgcn_readlane(v, 16)), max(__builtin_amdgcn_readlane(v, 32), __builtin_amdgcn_readlane(v, 48)));
    win = __builtin_amdgcn_readlane(wi_, __builtin_ctzll(__ballot(mine == kmax)));
    STAMP(12);
  }
  if (gu) {
    if (w == 0) {
      const u32 ho = O_H2 + win * 1280 + l * 16, ho2 = l < 16 ? ho + 1024 : NONE;
      u32x4 a, e;
#pragma nounroll
      for (int sp = 0; sp < SPIN; ++sp) {
        REREAD();
        a = ld16(rs, ho); e = ld16(rs, ho2);
        *(u32x4*)(L + L_H + l * 4) = a;
        if (l < 16) *(u32x4*)(L + L_H + 256 + l * 4) = e;
        if (!__any(max(umax4(a), umax4(e)) == SENT)) break;
        RETRY(0x10000);
      }
    }
    BAR();
    STAMP(13);
    f32x4 d = z4;
#pragma unroll
    for (int i = 0; i < 20; ++i) d = mfma16(*(const u32x4*)(L + L_H + i * 16 + gam * 4), wf[i], d);
    const float gg = bfr(d[0]), av = bfr(gg * sigm(gg)) * bfr(dppf<0x128>(d[0]));
    const float o = dppf<0xB1>(av);
    st4(rs, m16 < 8 ? O_ACT + (gam + 4 * (l & 1)) * ACT_REP + (xc * 6 + j) * 64 + g * 16 + (m16 >> 1) * 4 : NONE, (l & 1) ? pk(o, av) : pk(av, o));
  } else {
    const int dni = xc * 5 + j - 6;
    const rsrc_t ro = __builtin_amdgcn_make_buffer_rsrc(out, 0, S * D * 2, 0x00020000);
    const u32 oo = l < 8 ? (16 * (win & 15) + (win >> 4) + 256 * (kmax & 7)) * 1280 + dni * 32 + l * 4 : NONE;
    f32x2 base = {0, 0}, gs = {0, 0};
    if (w == 0) {
#pragma nounroll
      for (int sp = 0; sp < SPIN; ++sp) {
        REREAD();
        if (!__any(ld4(rs, O_DONE + win * 16) == SENT)) break;
      }
      REREAD();
      base = up2(ld4(ro, oo));
      const u32 rb = (kmax & 0x80000000u) ? (kmax & 0x7FFFFFFFu) : ~kmax;
      gs = up2(L[L_M + 960 + dni * 8 + (l & 7)]) * bfr(sigm(bfr(asf(rb))));
      keep(base); keep(gs);
    } else {
      __builtin_amdgcn_s_sleep(SLEEP_C);
    }
    const u32 ao = m16 < 12 ? O_ACT + xc * ACT_REP + (12 * g + m16) * 64 + gam * 16 : NONE;
    u32x4 a;
#pragma nounroll
    for (int sp = 0; sp < SPIN; ++sp) {
      REREAD();
      a = ld16(rs, ao);
      if (!__any(umax4(a) == SENT)) break;
      RETRY(0x10000);
    }
    STAMP(13);
    float s = 0;
#pragma unroll
    for (int r = 0; r < 4; ++r) {
      const f32x4 d0 = mfma16(a, wf[r], z4), d1 = mfma16(a, wf[4 + r], z4), d2 = mfma16(a, wf[8 + r], z4);
      s += gam == 0 ? d0[r] : (gam == 1 ? d1[r] : d2[r]);
    }
    s = fold32(s, s);
    s = fold16(s, s);
    if (l < 16) Lf[L_Q + 16 + g * 16 + l] = s;
    BAR();
    if (w == 0) {
      const float* p = Lf + L_Q + 16 + (l & 7) * 2;
      const f32x2 y = (*(const f32x2*)p + *(const f32x2*)(p + 16)) + (*(const f32x2*)(p + 32) + *(const f32x2*)(p + 48));
      const f32x2 o = base + gs * y;
      st4(ro, oo, pk(o[0], o[1]));
    }
  }
  STAMP(14);
  STAMP_OUT();
}

extern "C" void kernel_launch(void* const* d_in, const int* in_sizes, int n_in, void* d_out, int out_size, void* d_ws, size_t ws_size,
                              hipStream_t stream) {
  fused<<<256, 512, 0, stream>>>((const u16*)d_in[0], (const u16*)d_in[8], (const u16*)d_in[9], (char*)d_ws, (const u16*)d_in[1], (const u16*)d_in[2],
                                 (const u16*)d_in[4], (const u16*)d_in[5], (const u16*)d_in[6], (const u16*)d_in[7], (u16*)d_out);
}
```

```cpp
#include <hip/hip_runtime.h>
#include <stdint.h>

typedef unsigned u32;
typedef unsigned short u16;
typedef unsigned long long u64;
typedef u32 u32x2 __attribute__((ext_vector_type(2)));
typedef u32 u32x4 __attribute__((ext_vector_type(4)));
typedef u32 u32x2u __attribute__((ext_vector_type(2), aligned(2)));
typedef float f32x2 __attribute__((ext_vector_type(2)));
typedef float f32x4 __attribute__((ext_vector_type(4)));
typedef float f32x16 __attribute__((ext_vector_type(16)));
typedef short s16x4 __attribute__((ext_vector_type(4)));
typedef __bf16 bf16x2 __attribute__((ext_vector_type(2)));
typedef __bf16 bf16x8 __attribute__((ext_vector_type(8)));
typedef __amdgpu_buffer_rsrc_t rsrc_t;
#define DI __device__ __forceinline__
#define LAS __attribute__((address_space(3)))
#define GAS __attribute__((address_space(1)))
#define NWAVES 8

constexpr int S = 2048, D = 640, DF = 321, F = 1536;
constexpr float EPS = 1e-5f;
constexpr u32 SENT = 0xFFFFFFFFu, NONE = 0xFFFFFF00u;
constexpr int SPIN = 1 << 16;

constexpr u32 O_EPOCH = 0;
constexpr u32 O_TOK = 212992, TOK_REP = 2048;
constexpr u32 O_DONE = 8192;
constexpr u32 O_CAND = 16384, CAND_REP = 4096;
constexpr u32 O_ACT = 49152, ACT_REP = 4096;
constexpr u32 O_MOD = 81920, MOD_REP = 16384;
constexpr u32 O_H2 = 262144;
constexpr u32 SEGB = 16384 + 2304;
constexpr u32 O_T = 1u << 20;
constexpr u32 O_STAMP = 7u << 20;
constexpr u32 WS_USED = 8u << 20;

constexpr int L_U = 0;
constexpr int L_M = 2560;
constexpr int L_Q = 3840;
constexpr int L_H = 4352;
constexpr int L_G = 4672;
constexpr int L_T = 14912;
constexpr int L_Z = L_T;
constexpr int L_W = L_G;
constexpr int L_X1 = L_G + 4096;
constexpr int L_W2 = L_G + 8192;
constexpr int L_X2 = L_T + 5120;
constexpr int L_D = 35392;
constexpr int L_END = 35456;

DI u32 pk(float lo, float hi) { return __builtin_bit_cast(u32, __builtin_convertvector((f32x2){lo, hi}, bf16x2)); }
DI float asf(u32 v) { return __builtin_bit_cast(float, v); }
DI u32 asu(float v) { return __builtin_bit_cast(u32, v); }
DI float lo16(u32 v) { return asf(v << 16); }
DI float hi16(u32 v) { return asf(v & 0xffff0000u); }
DI f32x2 up2(u32 v) { return (f32x2){lo16(v), hi16(v)}; }
DI float bfr(float x) { return lo16(pk(x, 0.f)); }
DI float ldbf(const u16* p) { return lo16(*p); }
DI f32x16 mfma32(u32x4 a, u32x4 b, f32x16 c) {
  return __builtin_amdgcn_mfma_f32_32x32x16_bf16(__builtin_bit_cast(bf16x8, a), __builtin_bit_cast(bf16x8, b), c, 0, 0, 0);
}
DI f32x4 mfma16(u32x4 a, u32x4 b, f32x4 c) {
  return __builtin_amdgcn_mfma_f32_16x16x32_bf16(__builtin_bit_cast(bf16x8, a), __builtin_bit_cast(bf16x8, b), c, 0, 0, 0);
}
DI float dot2(u32 a, u32 b, float c) {
  return __builtin_amdgcn_fdot2_f32_bf16(__builtin_bit_cast(bf16x2, a), __builtin_bit_cast(bf16x2, b), c, false);
}
DI float cosr(float rev) { return __builtin_amdgcn_cosf(rev); }
DI float sinr(float rev) { return __builtin_amdgcn_sinf(rev); }
template <int CTRL> DI u32 dppu(u32 v) { return (u32)__builtin_amdgcn_update_dpp(0, (int)v, CTRL, 0xf, 0xf, true); }
template <int CTRL> DI float dppf(float v) { return asf(dppu<CTRL>(asu(v))); }
DI float row_sum(float v) {
  v += dppf<0xB1>(v); v += dppf<0x4E>(v); v += dppf<0x141>(v); v += dppf<0x140>(v);
  return v;
}
DI float fold32(float a, float b) { auto r = __builtin_amdgcn_permlane32_swap(asu(a), asu(b), false, false); return asf(r[0]) + asf(r[1]); }
DI float fold16(float a, float b) { auto r = __builtin_amdgcn_permlane16_swap(asu(a), asu(b), false, false); return asf(r[0]) + asf(r[1]); }
DI float rdl(float v, int lane) { return asf(__builtin_amdgcn_readlane(asu(v), lane)); }
DI float sigm(float x) { return __builtin_amdgcn_rcpf(1.f + __expf(-x)); }
DI u32 umax4(u32x4 v) { return max(max(v[0], v[1]), max(v[2], v[3])); }
template <class V> DI f32x2 pr(const V& v, int i) { return (f32x2){v[i], v[i + 1]}; }
DI void cmul2(f32x2 r, f32x2 im, f32x2 c, f32x2 s, u32& o0, u32& o1) {
  const f32x2 a = r * c - im * s, b = im * c + r * s;
  o0 = pk(a[0], b[0]); o1 = pk(a[1], b[1]);
}
DI u32x4 trfrag(const LAS u32* p, int hi) {
  const u32x2 a = __builtin_bit_cast(u32x2, __builtin_amdgcn_ds_read_tr16_b64_v4i16((LAS s16x4*)p));
  const u32x2 b = __builtin_bit_cast(u32x2, __builtin_amdgcn_ds_read_tr16_b64_v4i16((LAS s16x4*)(p + hi)));
  return (u32x4){a[0], a[1], b[0], b[1]};
}

DI u32x4 ld16(rsrc_t r, u32 o) { return __builtin_amdgcn_raw_buffer_load_b128(r, o, 0, 16); }
DI u32x2 ld8(rsrc_t r, u32 o) { return __builtin_amdgcn_raw_buffer_load_b64(r, o, 0, 16); }
DI u32 ld4(rsrc_t r, u32 o) { return __builtin_amdgcn_raw_buffer_load_b32(r, o, 0, 16); }
DI void st16(rsrc_t r, u32 o, u32x4 v) { __builtin_amdgcn_raw_buffer_store_b128(v, r, o, 0, 16); }
DI void st8(rsrc_t r, u32 o, u32x2 v) { __builtin_amdgcn_raw_buffer_store_b64(v, r, o, 0, 16); }
DI void st4(rsrc_t r, u32 o, u32 v) { __builtin_amdgcn_raw_buffer_store_b32(v, r, o, 0, 16); }
#define DRAIN() asm volatile("s_waitcnt vmcnt(0)" ::: "memory")
#define BAR() do { __builtin_amdgcn_sched_barrier(0); asm volatile("s_waitcnt lgkmcnt(0)\n\ts_barrier" ::: "memory"); __builtin_amdgcn_sched_barrier(0); } while (0)
#define REREAD() asm volatile("" ::: "memory")
#define PIN() __builtin_amdgcn_sched_barrier(0)
#define PRIO(n) __builtin_amdgcn_s_setprio(n)
template <class T> DI void keep(T& v) { asm volatile("" : "+v"(v)); }
DI void glds16(const void* base, u32 off, u32 lds_byte) {
  u32 m;
  asm volatile("s_mov_b32 %0, m0\n\ts_mov_b32 m0, %3\n\ts_nop 0\n\tglobal_load_lds_dwordx4 %1, %2\n\ts_mov_b32 m0, %0"
               : "=&s"(m) : "v"(off), "s"(base), "s"(lds_byte) : "memory");
}
DI void warm(u64 base, u32 bytes, u32 off, u32 lds_byte) {
  const u32x4 d = {(u32)base, (u32)(base >> 32) & 0xffffu, bytes, 0x00020000u};
  u32 m;
  asm volatile("s_mov_b32 %0, m0\n\ts_mov_b32 m0, %3\n\ts_nop 0\n\tbuffer_load_dword %1, %2, 0 offen lds\n\ts_mov_b32 m0, %0"
               : "=&s"(m) : "v"(off), "s"(d), "s"(lds_byte) : "memory");
}
#define STAMP(i) do { if (i == 0) { u64 t_; PIN(); asm volatile("s_memrealtime %0\n\ts_waitcnt lgkmcnt(0)" : "=s"(t_)::"memory"); PIN(); tdiag = (u32)t_; } \
    if (i == 3 && threadIdx.x == 0) ((volatile u32*)(ws + O_STAMP))[blockIdx.x] = tdiag; } while (0)
#define STAMP_OUT() do { if (blockIdx.x == 48 && w == 0) { const u32x4 q_ = ld16(rs, O_STAMP + l * 16); \
    int a_ = min(min((int)(q_[0] - tdiag), (int)(q_[1] - tdiag)), min((int)(q_[2] - tdiag), (int)(q_[3] - tdiag))); \
    int b_ = max(max((int)(q_[0] - tdiag), (int)(q_[1] - tdiag)), max((int)(q_[2] - tdiag), (int)(q_[3] - tdiag))); \
    for (int o_ = 1; o_ < 64; o_ <<= 1) { a_ = min(a_, __shfl_xor(a_, o_)); b_ = max(b_, __shfl_xor(b_, o_)); } \
    const u32 mn = tdiag + a_, mx = tdiag + b_; const u32 d_ = __builtin_amdgcn_readfirstlane(min(4u * (mx - mn), 5000u)); \
    const u64 t0_ = __builtin_amdgcn_s_memrealtime(); \
    while ((u32)(__builtin_amdgcn_s_memrealtime() - t0_) < d_) {} } } while (0)
#define RETRY(v)

#define WFIRST 1
#define SLEEP_M 0
#define SLEEP_T 12
#define SLEEP_M2 16
#define SLEEP_NC 0
#define SLEEP_C 40

__global__ __launch_bounds__(512, 2) void fused(const u16* __restrict__ x, const u16* __restrict__ cvec, const u16* __restrict__ wmod, char* ws,
                                                const u16* __restrict__ fwr, const u16* __restrict__ fwi, const u16* __restrict__ wrt,
                                                const u16* __restrict__ wg, const u16* __restrict__ wu, const u16* __restrict__ wd, u16* out) {
  __shared__ __attribute__((aligned(16))) u32 L[L_END];
  u32 tdiag = 0;
  STAMP(0);
  u64 code_ptr;
  u32 code_len;
  asm volatile("s_getpc_b64 %0\n.Lcode_from:\n\ts_mov_b32 %1, .Lcode_to-.Lcode_from-4\n\t.subsection 1\n.Lcode_to:\n\t.subsection 0"
               : "=s"(code_ptr), "=s"(code_len));
  const u32 lds0 = (u32)(size_t)(LAS u32*)L, ldsd = lds0 + L_D * 4;
  warm(code_ptr, code_len, (threadIdx.x * 4 + ((blockIdx.x >> 3) & 3)) * 128u, ldsd);
  asm volatile("" ::"s"(x), "s"(cvec), "s"(wmod), "s"(ws), "s"(fwr), "s"(fwi), "s"(wrt), "s"(wg), "s"(wu), "s"(wd), "s"(out));
  const int b = blockIdx.x, xc = b & 7, j = b >> 3, t = threadIdx.x, l = t & 63;
  const int w = __builtin_amdgcn_readfirstlane(t >> 6), w4 = w & 3;
  const int pos = xc * 32 + j, s2 = 16 * (pos & 15) + (pos >> 4);
  const bool col = j >= 11, hasmod = j < 10;
  const int seg = xc * 21 + j - 11, mb = xc * 10 + j;
  const int m16 = l & 15, gam = l >> 4;
  const rsrc_t rs = __builtin_amdgcn_make_buffer_rsrc(ws, 0, WS_USED, 0x00020000);
  float* Lf = (float*)L;

  u32x2 xa[2], xb[2];
  u32 xd[2];
  u32x4 cf4;
  u64 tag = *(const __attribute__((address_space(4))) u64*)(size_t)(ws + O_EPOCH);
  if (w >= 4) {
#pragma unroll
    for (int i = 0; i < 2; ++i) {
      const u16* xrow = x + (size_t)(s2 + 256 * w4 + 1024 * i) * D;
      xa[i] = *(const u32x2*)(xrow + 4 * l); xb[i] = *(const u32x2*)(xrow + 256 + 4 * l); xd[i] = *(const u32*)(xrow + 512 + 2 * l);
    }
  }
  if (w < 4 && hasmod) {
    asm volatile("global_load_dwordx4 %0, %1, %2" : "=v"(cf4) : "v"(320 * w4 + 64 * min(m16, 4) + 16 * gam), "s"(cvec) : "memory");
    const u32 o = (160 * w4 + (l >> 1)) * 7680 + mb * 32 + (l & 1) * 16;
    int k = mb & 3;
#pragma unroll
    for (int i = 0; i < 5; ++i) { glds16(wmod, o + k * (32 * 7680), lds0 + (L_G + w4 * 2560 + k * 256) * 4); k = k == 4 ? 0 : k + 1; }
  }
  if (w < 2 && col) warm((u64)wmod, 640 * 7680, (t & 3) < 3 ? (32 * (j - 11) + (t >> 2)) * 7680 + 320 * xc + (t & 3) * 156 : NONE, ldsd);
  PIN();
  int k1 = 0, kap0 = 0, lam0 = 0;
  if (col) {
    if (seg < 48) { const int c0 = (seg % 6) * 4; k1 = c0 >= 12 ? 4 : 0; kap0 = c0 >= 12 ? c0 - 12 : c0; lam0 = 4 * (seg / 6); }
    else { k1 = 1 + (seg - 48) / 40; kap0 = ((seg - 48) % 5) * 4; lam0 = 4 * (((seg - 48) % 40) / 5); }
  }
  const bool mir = lam0 >= 16, herm = col && kap0 == 0 && (lam0 & 15) == 0;

  if (w >= 4) {
    const int tr = t - 256;
    u32x4 B1r[4], B1i[4], A4r[4], A4i[4], Fr, Fi, Gr, Gi;
    if (col) {
      const float pf = (float)m16 * (1.f / 256.f), af = (float)(16 * ((m16 >> 2) + 4 * (m16 & 3)) + 4 * w4) * (1.f / 256.f);
#pragma unroll
      for (int q = 0; q < 4; ++q) {
#pragma unroll
        for (int bl = 0; bl < 4; ++bl) {
          const float t1 = (float)(16 * gam + 4 * w4 + 64 * q + bl) * pf, c1 = cosr(t1), s1 = sinr(t1);
          B1r[bl][q] = pk(c1, s1); B1i[bl][q] = pk(-s1, c1);
          const float t4 = (float)(4 * gam + q) * (af + (float)bl * (1.f / 256.f)), c4 = cosr(t4), s4 = sinr(t4);
          A4r[bl][q] = pk(c4, -s4); A4i[bl][q] = pk(s4, c4);
        }
        const float a = (float)(4 * gam + q) * (16.f * pf), cs = cosr(a), sn = sinr(a);
        Fr[q] = pk(cs, sn); Fi[q] = pk(-sn, cs); Gr[q] = pk(cs, -sn); Gi[q] = pk(sn, cs);
      }
#pragma unroll
      for (int q = 0; q < 4; ++q) { keep(B1r[q]); keep(B1i[q]); keep(A4r[q]); keep(A4i[q]); }
      keep(Fr); keep(Fi); keep(Gr); keep(Gi);
    }
    PIN();
    f32x2 xv[2][5];
    {
      float s[2], q[2];
#pragma unroll
      for (int i = 0; i < 2; ++i) {
        xv[i][0] = up2(xa[i][0]); xv[i][1] = up2(xa[i][1]); xv[i][2] = up2(xb[i][0]); xv[i][3] = up2(xb[i][1]); xv[i][4] = up2(xd[i]);
        f32x2 a = xv[i][0], c = xv[i][0] * xv[i][0];
#pragma unroll
        for (int k = 1; k < 5; ++k) { a += xv[i][k]; c += xv[i][k] * xv[i][k]; }
        s[i] = a[0] + a[1]; q[i] = c[0] + c[1];
      }
      const float v = row_sum(fold16(fold32(s[0], s[1]), fold32(q[0], q[1]))) * (1.f / D);
      const float m0 = rdl(v, 0), m1 = rdl(v, 32);
      const float r0 = __builtin_amdgcn_rsqf(fmaxf(rdl(v, 16) - m0 * m0, 0.f) + EPS), r1 = __builtin_amdgcn_rsqf(fmaxf(rdl(v, 48) - m1 * m1, 0.f) + EPS);
      f32x2 sm[5], df[5];
#pragma unroll
      for (int k = 0; k < 5; ++k) { const f32x2 z0 = (xv[0][k] - m0) * r0, z1 = (xv[1][k] - m1) * r1; sm[k] = z0 + z1; df[k] = z0 - z1; }
      float* z = Lf + L_Z + w4 * 640;
      *(f32x4*)(z + 4 * l) = (f32x4){sm[0][0], sm[0][1], sm[1][0], sm[1][1]};
      *(f32x4*)(z + 256 + 4 * l) = (f32x4){sm[2][0], sm[2][1], sm[3][0], sm[3][1]};
      *(f32x2*)(z + 512 + 2 * l) = sm[4];
      *(f32x4*)(z + 2560 + 4 * l) = (f32x4){df[0][0], df[0][1], df[1][0], df[1][1]};
      *(f32x4*)(z + 2560 + 256 + 4 * l) = (f32x4){df[2][0], df[2][1], df[3][0], df[3][1]};
      *(f32x2*)(z + 2560 + 512 + 2 * l) = df[4];
    }
    if (tr == 0) *(u32x2*)(L + L_Q) = (u32x2){0u, 0u};
    STAMP(1);
    asm volatile("" : "+s"(tag)::"memory");
    PRIO(2);
    BAR();
    const u32 tlo = (u32)(tag + 1), thi = (u32)((tag + 1) >> 32);
    {
      const int h = l >> 5, lp = l & 31;
      const float* z = Lf + L_Z + h * 2560 + 160 * w4;
      const float e = h ? 0.f : 1.f, kk = h ? 0.70710678f : 0.f, ek = e + kk, sg = h ? -1.f : 1.f, ne = 1.f - e;
      f32x4 z4[4]; float z1[4];
#pragma unroll
      for (int i = 0; i < 4; ++i) { z4[i] = *(const f32x4*)(z + i * 640 + 4 * lp); z1[i] = z[i * 640 + 128 + lp]; }
      f32x2 p0[5], p1[5];
#pragma unroll
      for (int k = 0; k < 5; ++k) {
        const float a = k < 4 ? z4[0][k] : z1[0], c = k < 4 ? z4[1][k] : z1[1], bb = k < 4 ? z4[2][k] : z1[2], d = k < 4 ? z4[3][k] : z1[3];
        const float sm = c + d, df = c - d;
        p0[k] = (f32x2){a + e * (bb + sm) + kk * df, e * a + sg * bb - ek * sm};
        p1[k] = (f32x2){a - e * bb - kk * df, ne * bb - kk * sm - e * df};
        keep(p0[k]); keep(p1[k]);
      }
      if (SLEEP_M) __builtin_amdgcn_s_sleep(SLEEP_M);
      const u32 mo = O_MOD + xc * MOD_REP + 640 * w4;
      const u32 o4 = mo + 16 * lp, o1 = mo + 512 + 8 * (lp >> 1), want = (lp & 2) ? thi : tlo;
      const u32 hm = h ? 0u : ~0u;
      u32* u = L + L_U + h * 640 + 5 * w4;
      u32* ua = u + (lp & 7) * 20 + (lp >> 3);
      u32* ub = u + ((lp >> 2) + 8 * (lp & 3)) * 20 + 4;
      u32x4 c4, s4; u32x2 c1, s1;
      STAMP(2);
#pragma nounroll
      for (int sp = 0; sp < SPIN; ++sp) {
        REREAD();
        c4 = ld16(rs, o4 + 2560); c1 = ld8(rs, o1 + 2560);
        s4 = ld16(rs, h ? NONE : o4); s1 = ld8(rs, h ? NONE : o1);
        const u32 bad = (c4[1] ^ tlo) | (c4[3] ^ thi) | (c1[1] ^ want) | (((s4[1] ^ tlo) | (s4[3] ^ thi) | (s1[1] ^ want)) & hm);
        if (!__any(bad != 0)) break;
        RETRY(0x100);
      }
      STAMP(3);
      const float A[5] = {lo16(c4[0]), hi16(c4[0]), lo16(c4[2]), hi16(c4[2]), (lp & 1) ? hi16(c1[0]) : lo16(c1[0])};
      const float B[5] = {lo16(s4[0]), hi16(s4[0]), lo16(s4[2]), hi16(s4[2]), (lp & 1) ? hi16(s1[0]) : lo16(s1[0])};
#pragma unroll
      for (int k = 0; k < 5; ++k) {
        const f32x2 q1 = p1[k] * A[k];
        u32* o = k < 4 ? ua + k * 160 : ub;
        o[0] = pk(p0[k][0] * A[k] + B[k], p0[k][1] * A[k]);
        o[1280] = pk(q1[0], q1[1]);
      }
    }
    BAR();
    PRIO(0);
    STAMP(4);
    if (w4 < 3) warm((u64)wmod, 640 * 7680, tr < 160 && (tr & 7) < 5 ? (20 * j + (tr >> 3)) * 7680 + 2560 + 640 * xc + 128 * (tr & 7) : NONE, ldsd);
    if (w4 == 3 && col) {
      __builtin_amdgcn_s_sleep(SLEEP_T);
#pragma nounroll
      for (int sp = 0; sp < SPIN; ++sp) {
        REREAD();
        const u32x4 g = ld16(rs, O_TOK + xc * TOK_REP + l * 16), e = ld16(rs, O_TOK + xc * TOK_REP + 1024 + l * 16);
        if (!__any(((g[0] ^ tlo) | (g[1] ^ thi) | (g[2] ^ tlo) | (g[3] ^ thi) | (e[0] ^ tlo) | (e[1] ^ thi) | (e[2] ^ tlo) | (e[3] ^ thi)) != 0)) break;
        __builtin_amdgcn_s_sleep(8);
        RETRY(0x10000);
      }
    }
    STAMP(5);
    BAR();
    STAMP(6);
    if (col) {
      PRIO(2);
      const u32 o = O_T + seg * SEGB + w4 * 4096 + l * 4;
      u32x4 in[4];
#pragma unroll
      for (int i = 0; i < 16; ++i) in[i >> 2][i & 3] = ld4(rs, o + i * 256);
      PIN();
      f32x4 fr[4], fi[4];
      {
        const bool hw = herm && w4 == 0;
        const float h0 = ((k1 & 3) == 0 && kap0 == 0 ? 0.5f : 1.f) * (hw ? 0.5f : 1.f), h2 = (k1 & 3) == 0 && kap0 == 8 ? 0.5f : 1.f;
        u32x4 v[4]; u32 e[4];
#pragma unroll
        for (int r = 0; r < 4; ++r) {
          v[r] = *(const u32x4*)(L + L_W + (m16 + 16 * (4 * gam + r)) * 16 + w4 * 4);
          e[r] = L[L_W2 + m16 + 16 * (4 * gam + r)] & (hw ? ~0u : 0u);
        }
#pragma unroll
        for (int r = 0; r < 4; ++r) {
#pragma unroll
          for (int c = 0; c < 4; ++c) { fr[c][r] = lo16(v[r][c]); fi[c][r] = hi16(v[r][c]); }
          fr[0][r] = (fr[0][r] + lo16(e[r])) * h0; fi[0][r] = (fi[0][r] + hi16(e[r])) * h0;
          fr[2][r] *= h2; fi[2][r] *= h2;
        }
      }
#pragma unroll
      for (int c = 0; c < 4; ++c) { keep(fr[c]); keep(fi[c]); }
      PIN();
      const f32x4 z = {0, 0, 0, 0};
      f32x4 yr[4], yi[4];
#pragma nounroll
      for (int sp = 0; sp < SPIN; ++sp) {
#pragma unroll
        for (int bl = 0; bl < 4; ++bl) { yr[bl] = mfma16(in[bl], B1r[bl], z); yi[bl] = mfma16(in[bl], B1i[bl], z); }
        if (!__any(max(max(umax4(in[0]), umax4(in[1])), max(umax4(in[2]), umax4(in[3]))) == SENT)) break;
        REREAD();
#pragma unroll
        for (int i = 0; i < 16; ++i) in[i >> 2][i & 3] = ld4(rs, o + i * 256);
        RETRY(1);
      }
      STAMP(7);
      u32* x1w = L + L_X1 + gam * 1024 + w4 * 64 + m16 * 4;
#pragma unroll
      for (int q = 0; q < 4; ++q)
        *(u32x4*)(x1w + q * 256) = (u32x4){pk(yr[0][q], yi[0][q]), pk(yr[1][q], yi[1][q]), pk(yr[2][q], yi[2][q]), pk(yr[3][q], yi[3][q])};
      BAR();
      u32x4 a[4];
#pragma unroll
      for (int c = 0; c < 4; ++c) a[c] = *(const u32x4*)(L + L_X1 + c * 1024 + w4 * 256 + l * 4);
#pragma unroll
      for (int c = 0; c < 4; ++c) { yr[c] = mfma16(Fr, a[c], z); yi[c] = mfma16(Fi, a[c], z); }
#pragma unroll
      for (int c = 0; c < 4; ++c) {
        u32 o0, o1, o2, o3;
        cmul2(pr(yr[c], 0), pr(yi[c], 0), pr(fr[c], 0), pr(fi[c], 0), o0, o1);
        cmul2(pr(yr[c], 2), pr(yi[c], 2), pr(fr[c], 2), pr(fi[c], 2), o2, o3);
        a[c] = (u32x4){o0, o1, o2, o3};
        yr[c] = mfma16(a[c], Gr, z); yi[c] = mfma16(a[c], Gi, z);
      }
      u32* x2w = L + L_X2 + m16 * 256 + gam * 64;
#pragma unroll
      for (int c = 0; c < 4; ++c)
        *(u32x4*)(x2w + (((4 * c + w4) ^ m16) << 2)) =
            (u32x4){pk(yr[c][0], yi[c][0]), pk(yr[c][1], yi[c][1]), pk(yr[c][2], yi[c][2]), pk(yr[c][3], yi[c][3])};
      BAR();
#pragma unroll
      for (int bl = 0; bl < 4; ++bl) a[bl] = *(const u32x4*)(L + L_X2 + (4 * w4 + bl) * 256 + gam * 64 + ((m16 ^ (4 * w4 + bl)) << 2));
#pragma unroll
      for (int bl = 0; bl < 4; ++bl) { yr[bl] = mfma16(A4r[bl], a[bl], z); yi[bl] = mfma16(A4i[bl], a[bl], z); }
#pragma unroll
      for (int i = 0; i < 16; ++i) st4(rs, o + i * 256, ~(pk(yi[i >> 2][i & 3], yr[i >> 2][i & 3]) ^ in[i >> 2][i & 3]));
      PRIO(0);
      STAMP(8);
      DRAIN();
      STAMP(9);
      BAR();
    } else {
      BAR();
      __builtin_amdgcn_s_sleep(SLEEP_M2);
    }
    f32x2 ga[5], cb[5], sb[5], gb[5], vv[5];
    float sv, sbw;
    {
      const u32 mo = O_MOD + xc * MOD_REP + 5120 + w4 * 2560 + l * 16;
      const u32x2 wra = *(const u32x2*)(wrt + 4 * l), wrb = *(const u32x2*)(wrt + 256 + 4 * l);
      const u32 wrd = *(const u32*)(wrt + 512 + 2 * l);
      u32x4 g0, g1, g2;
#pragma nounroll
      for (int sp = 0; sp < SPIN; ++sp) {
        REREAD();
        g0 = ld16(rs, mo); g1 = ld16(rs, mo + 1024); g2 = ld16(rs, l < 32 ? mo + 2048 : NONE);
        const u32 bad = (g0[1] ^ tlo) | (g0[3] ^ thi) | (g1[1] ^ tlo) | (g1[3] ^ thi) | (l < 32 ? (g2[1] ^ tlo) | (g2[3] ^ thi) : 0u);
        if (!__any(bad != 0)) break;
        __builtin_amdgcn_s_sleep(4);
        RETRY(0x10000);
      }
      u32* lm = L + L_M + w4 * 320 + 2 * l;
      *(u32x2*)lm = (u32x2){g0[0], g0[2]}; *(u32x2*)(lm + 128) = (u32x2){g1[0], g1[2]};
      if (l < 32) *(u32x2*)(lm + 256) = (u32x2){g2[0], g2[2]};
      BAR();
      auto ld5 = [&](const u32* p, f32x2 (&v)[5]) {
        const u32x2 a = *(const u32x2*)(p + 2 * l), c = *(const u32x2*)(p + 128 + 2 * l);
        v[0] = up2(a[0]); v[1] = up2(a[1]); v[2] = up2(c[0]); v[3] = up2(c[1]); v[4] = up2(p[256 + l]);
      };
      ld5(L + L_M, ga); ld5(L + L_M + 320, sb); ld5(L + L_M + 640, cb); ld5(L + L_M + 960, gb);
      vv[0] = up2(wra[0]); vv[1] = up2(wra[1]); vv[2] = up2(wrb[0]); vv[3] = up2(wrb[1]); vv[4] = up2(wrd);
      f32x2 a = {0, 0}, c = {0, 0};
#pragma unroll
      for (int k = 0; k < 5; ++k) { c += sb[k] * vv[k]; vv[k] *= cb[k]; a += vv[k]; ga[k] *= 1.f / ((float)S * (float)D); }
      const float v = fold32(a[0] + a[1], c[0] + c[1]), v2 = row_sum(fold16(v, v));
      sv = rdl(v2, 0); sbw = rdl(v2, 32);
    }
    u32 cf[4];
    cf[0] = pk(2.f, (w4 & 1) ? -2.f : 2.f);
#pragma unroll
    for (int g = 1; g < 4; ++g) { const float th = (float)(w4 * g) * 0.125f; cf[g] = pk(2.f * cosr(th), -2.f * sinr(th)); }
#pragma unroll
    for (int k = 0; k < 5; ++k) { keep(ga[k]); keep(cb[k]); keep(sb[k]); keep(gb[k]); keep(vv[k]); keep(xv[0][k]); keep(xv[1][k]); }
#pragma unroll
    for (int g = 0; g < 4; ++g) keep(cf[g]);
    u32 krow = w4 + 4 * (l >> 5);
    keep(krow); keep(sv); keep(sbw);
    if (!col) {
      if (j < 6) {
        const u32 o = (160 * w4 + (l >> 2)) * (F * 2) + (xc * 6 + j) * 64 + (l & 3) * 16;
#pragma nounroll
        for (int i = 0; i < 10; ++i) {
          glds16(wg, o + i * (16 * F * 2), lds0 + (L_T + w4 * 2560 + i * 256) * 4);
          glds16(wu, o + i * (16 * F * 2), lds0 + (L_T + 10240 + w4 * 2560 + i * 256) * 4);
        }
      } else {
        const u32 o = (384 * w4 + (l >> 1)) * (D * 2) + (xc * 5 + j - 6) * 32 + (l & 1) * 16;
        int k = 3 * (j & 3);
#pragma nounroll
        for (int i = 0; i < 12; ++i) { glds16(wd, o + k * (32 * D * 2), lds0 + (L_T + w4 * 3072 + k * 256) * 4); k = k == 11 ? 0 : k + 1; }
      }
    }
    STAMP(10);
    PRIO(2);
    BAR();
    STAMP(11);
    u32x4 ea[4], eb[4]; u32x2 ec[4];
#pragma unroll
    for (int g = 0; g < 4; ++g) {
      const u32* e = L + L_U + g * 640;
      ea[g] = *(const u32x4*)(e + 4 * l); eb[g] = *(const u32x4*)(e + 256 + 4 * l); ec[g] = *(const u32x2*)(e + 512 + 2 * l);
    }
    PIN();
    f32x2 sa_[2] = {}, qa[2] = {}, da[2] = {};
#pragma unroll
    for (int k = 0; k < 5; ++k) {
      f32x2 ev, od;
#pragma unroll
      for (int i = 0; i < 2; ++i) {
        auto el = [&](int g) { return k < 2 ? ea[g][2 * k + i] : (k < 4 ? eb[g][2 * k - 4 + i] : ec[g][i]); };
        ev[i] = dot2(el(2), cf[2], dot2(el(0), cf[0], 0.f));
        od[i] = dot2(el(3), cf[3], dot2(el(1), cf[1], 0.f));
      }
      xv[0][k] += ga[k] * (ev + od); xv[1][k] += ga[k] * (ev - od);
#pragma unroll
      for (int i = 0; i < 2; ++i) { sa_[i] += xv[i][k]; qa[i] += xv[i][k] * xv[i][k]; da[i] += xv[i][k] * vv[k]; }
    }
    const float dsum = fold32(da[0][0] + da[0][1], da[1][0] + da[1][1]);
    const float sq = row_sum(fold16(fold32(sa_[0][0] + sa_[0][1], sa_[1][0] + sa_[1][1]), fold32(qa[0][0] + qa[0][1], qa[1][0] + qa[1][1]))) * (1.f / D);
    const float dt = row_sum(fold16(dsum, dsum));
    const auto sw = __builtin_amdgcn_permlane16_swap(asu(sq), asu(sq), false, false);
    const float mean = asf(sw[0]), rstd = __builtin_amdgcn_rsqf(fmaxf(asf(sw[1]) - mean * mean, 0.f) + EPS);
    const u32 rb = asu(rstd * (dt - mean * sv) + sbw);
    const u32 key = (min(rb ^ ((u32)((int)rb >> 31) | 0x80000000u), 0xFFFFFFF0u) & ~7u) | krow;
    if ((l & 31) == 0) __hip_atomic_fetch_max(L + L_Q, key, __ATOMIC_RELAXED, __HIP_MEMORY_SCOPE_WORKGROUP);
    BAR();
    PRIO(0);
    if (!col) {
      DRAIN();
      BAR();
    }
    STAMP(12);
    const int best = __builtin_amdgcn_readfirstlane(L[L_Q]) & 7;
    const rsrc_t ro = __builtin_amdgcn_make_buffer_rsrc(out, 0, S * D * 2, 0x00020000);
#pragma unroll
    for (int i = 0; i < 2; ++i) {
      const float m = rdl(mean, 32 * i), rr = rdl(rstd, 32 * i);
      u32 hp[5], op[5];
#pragma unroll
      for (int k = 0; k < 5; ++k) {
        const f32x2 h2 = (xv[i][k] - m) * rr * cb[k] + sb[k], o = xv[i][k] + gb[k] * h2;
        hp[k] = pk(h2[0], h2[1]); op[k] = pk(o[0], o[1]);
      }
      if (best == w4 + 4 * i) {
        const u32 ho = O_H2 + pos * 1280;
        st8(rs, ho + 8 * l, (u32x2){hp[0], hp[1]}); st8(rs, ho + 512 + 8 * l, (u32x2){hp[2], hp[3]}); st4(rs, ho + 1024 + 4 * l, hp[4]);
      }
      const u32 oo = (s2 + 256 * w4 + 1024 * i) * 1280;
      st8(ro, oo + 8 * l, (u32x2){op[0], op[1]}); st8(ro, oo + 512 + 8 * l, (u32x2){op[2], op[3]}); st4(ro, oo + 1024 + 4 * l, op[4]);
    }
    if ((best & 3) == w4) {
      DRAIN();
      if (l == 0) st4(rs, O_DONE + pos * 16, 1u);
    }
    if (b == 255 && tr == 0) st8(rs, O_EPOCH, (u32x2){tlo, thi});
    STAMP(13);
    STAMP_OUT();
    return;
  }

  const int g = w, c = l & 31, h = l >> 5;
  const int nsg = g == 0 ? 6 : 5;
  const bool act = c < 4 * nsg;
  const u32 slot = act ? O_T + ((g == 0 ? 0 : 8 + 40 * g) + h * nsg + (c >> 2)) * SEGB + pos * 64 + (c & 3) * 16 : NONE;
  const u32 sstep = act ? 2 * nsg * SEGB : 0;
  const f32x4 z4 = {0, 0, 0, 0};
  u32x4 old[4], ar[4], ai[4], cr[3], ci[3];
  f32x2 uc[6], us[6];
  u32 tlo, thi;
  auto publish_mod = [&](const float* p, int col0) {
    const float s0 = p[l] + p[64 + l], s1 = fold32(s0, s0);
    float s = bfr(fold16(s1, s1));
    s = (col0 >= 640 && col0 < 1280) || (col0 >= 2560 && col0 < 3200) ? 1.f + s : (col0 < 640 ? 8.f * s : s);
    const float o = dppf<0xB1>(s);
    st8(rs, O_MOD + (gam + 4 * (l & 1)) * MOD_REP + col0 * 4 + (m16 >> 1) * 8, (u32x2){(l & 1) ? pk(o, s) : pk(s, o), (l & 2) ? thi : tlo});
  };
  {
    u32x2 wa[4], wc[4];
    const int kd = kap0 + 20 * (lam0 + (t & 3)), cw = mir ? D - 3 - kd : kd;
    auto ldw = [&](int i) {
      const int ks = k1 + 8 * (((t >> 2) + 64 * (i + seg)) & 255);
      const u32 o = ((mir ? (S - ks) & (S - 1) : ks) * DF + cw) * 2;
      wa[i] = *(const u32x2u*)((const char*)fwr + o); wc[i] = *(const u32x2u*)((const char*)fwi + o);
    };
    if (col) {
#pragma unroll
      for (int i = 0; i < WFIRST; ++i) ldw(i);
    }
    PIN();
    const float cf = (float)c;
#pragma unroll
    for (int tau = 0; tau < 3; ++tau) {
      const bool up = g == 0 && (tau == 2 || (tau == 1 && h));
      float kb = (float)(8 * tau + 4 * h - (up ? 12 : 0)), t1 = (float)(s2 * (g ? g : (up ? 4 : 0))) * (1.f / 2048.f);
      u32 mr = up ? 0u : ~0u, si = g ? 0x07060504u : (up ? 0x03020100u : 0x0c0c0c0cu);
      keep(kb); keep(t1); keep(mr); keep(si);
#pragma unroll
      for (int q = 0; q < 4; ++q) {
        const float kq = kb + (float)q;
        const float th = kq * (cf * (1.f / 640.f)) + t1;
        uc[2 * tau + (q >> 1)][q & 1] = cosr(th); us[2 * tau + (q >> 1)][q & 1] = sinr(th);
        const float t2 = kq * (cf * 0.05f), c2 = cosr(t2), s2_ = sinr(t2);
        const u32 p = pk(c2, -s2_);
        cr[tau][q] = p & mr;
        ci[tau][q] = __builtin_amdgcn_perm(pk(s2_, c2), p, si);
      }
    }
#pragma unroll
    for (int i = 0; i < 3; ++i) { keep(cr[i]); keep(ci[i]); }
#pragma unroll
    for (int i = 0; i < 6; ++i) { keep(uc[i]); keep(us[i]); }
    PIN();
    if (hasmod) {
      asm volatile("s_waitcnt vmcnt(5)" : "+v"(cf4)::"memory");
#pragma unroll
      for (int q = 0; q < 4; ++q) { const float a = lo16(cf4[q]), e = hi16(cf4[q]); cf4[q] = pk(a * sigm(a), e * sigm(e)); }
      keep(cf4);
    }
    {
      const u32x4 sent = {SENT, SENT, SENT, SENT};
      const u32 o = O_T + (t >> 2) * SEGB + pos * 64 + (t & 3) * 16;
      st16(rs, o, sent);
      st16(rs, o + 64 * SEGB, sent);
      const u32 oh = O_H2 + pos * 1280 + (t - 160) * 16, oc = (t < 248 ? O_CAND + (t - 240) * CAND_REP : O_DONE) + pos * 16;
      st16(rs, t < 160 ? o + 128 * SEGB : (t < 240 ? oh : oc), sent);
      st16(rs, t < 32 && j < 6 ? O_ACT + (t >> 2) * ACT_REP + (xc * 6 + j) * 64 + (t & 3) * 16 : NONE, sent);
    }
    PIN();
    u32 hr, hi;
    if (col) {
#pragma unroll
      for (int i = WFIRST; i < 4; ++i) ldw(i);
      const int ks = k1 + 8 * t;
      const u32 o2 = herm ? ((mir ? ks : (S - ks) & (S - 1)) * DF + 20 * lam0) * 2 : NONE;
      hr = (u32)__builtin_amdgcn_raw_buffer_load_b16(__builtin_amdgcn_make_buffer_rsrc((void*)fwr, 0, S * DF * 2, 0x00020000), o2, 0, 0);
      hi = (u32)__builtin_amdgcn_raw_buffer_load_b16(__builtin_amdgcn_make_buffer_rsrc((void*)fwi, 0, S * DF * 2, 0x00020000), o2, 0, 0);
    }
    PIN();
    STAMP(1);
    if (hasmod) {
      asm volatile("s_waitcnt vmcnt(4)" ::: "memory");
      STAMP(2);
      const LAS u32* p = (const LAS u32*)(L + L_G) + g * 2560 + gam * 64 + (m16 >> 2) * 8 + (m16 & 3) * 2;
      f32x4 d[5];
#pragma unroll
      for (int i = 0; i < 5; ++i) d[i] = mfma16(cf4, trfrag(p + i * 256, 32), z4);
      if (l < 32) Lf[L_Q + 16 + g * 32 + l] = gam ? d[4][0] : (d[0][0] + d[1][1]) + (d[2][2] + d[3][3]);
    }
    asm volatile("" : "+s"(tag)::"memory");
    tlo = (u32)(tag + 1); thi = (u32)((tag + 1) >> 32);
    BAR();
    if (w == 0 && hasmod) publish_mod(Lf + L_Q + 16, mb * 16);
    STAMP(3);
    const float kapf = (float)((g == 0 && c >= 12) ? c - 12 : c);
    const float k1f = g ? (float)g : (c >= 12 ? 4.f : 0.f);
    u32 sel = g ? 0x03020100u : (c < 11 ? 0x0c0c0100u : (c >= 12 && c < 23 ? 0x01000c0cu : 0x0c0c0c0cu)), sel2 = h ? 0x0c0c0c0cu : sel;
    keep(sel); keep(sel2);
    u32x4 br[3], bi[3];
    f32x2 tc[8], ts[8];
#pragma unroll
    for (int tau = 0; tau < 3; ++tau)
#pragma unroll
      for (int q = 0; q < 4; ++q) {
        const float th = (float)(8 * tau + 4 * h + q) * (kapf * 0.05f);
        const float cs = cosr(th), sn = sinr(th);
        br[tau][q] = __builtin_amdgcn_perm(0u, pk(cs, sn), tau == 2 ? sel2 : sel);
        bi[tau][q] = __builtin_amdgcn_perm(0u, pk(-sn, cs), tau == 2 ? sel2 : sel);
      }
    const float t0 = (float)s2 * k1f * (-1.f / 2048.f);
#pragma unroll
    for (int tau = 0; tau < 4; ++tau)
#pragma unroll
      for (int q = 0; q < 4; ++q) {
        const float n2 = (float)(8 * tau + 4 * h + q);
        const float th = n2 * (kapf * (-1.f / 640.f)) + t0;
        tc[2 * tau + (q >> 1)][q & 1] = cosr(th); ts[2 * tau + (q >> 1)][q & 1] = sinr(th);
        const float t2 = n2 * ((float)c * (1.f / 32.f)), c2 = cosr(t2), s2_ = sinr(t2);
        ar[tau][q] = pk(c2, s2_); ai[tau][q] = pk(-s2_, c2);
      }
#pragma unroll
    for (int i = 0; i < 3; ++i) { keep(br[i]); keep(bi[i]); }
#pragma unroll
    for (int i = 0; i < 4; ++i) { keep(ar[i]); keep(ai[i]); }
#pragma unroll
    for (int i = 0; i < 8; ++i) { keep(tc[i]); keep(ts[i]); }
    if (col) {
#pragma unroll
      for (int i = 0; i < 4; ++i) {
        u32x4 v = {__builtin_amdgcn_perm(wc[i][0], wa[i][0], 0x05040100), __builtin_amdgcn_perm(wc[i][0], wa[i][0], 0x07060302),
                   __builtin_amdgcn_perm(wc[i][1], wa[i][1], 0x05040100), __builtin_amdgcn_perm(wc[i][1], wa[i][1], 0x07060302)};
        if (mir) v = (u32x4){v[3] ^ 0x80000000u, v[2] ^ 0x80000000u, v[1] ^ 0x80000000u, v[0] ^ 0x80000000u};
        *(u32x4*)(L + L_W + ((t + 256 * (i + seg)) & 1023) * 4) = v;
      }
      keep(hr); keep(hi);
      L[L_W2 + t] = __builtin_amdgcn_perm(hi, hr, 0x05040100) ^ (mir ? 0u : 0x80000000u);
    }
    DRAIN();
    if (__builtin_amdgcn_readfirstlane(l == 0 ? __hip_atomic_fetch_add(L + L_Q + 1, 1u, __ATOMIC_RELAXED, __HIP_MEMORY_SCOPE_WORKGROUP) : 0u) == 3u)
      st8(rs, l < 8 ? O_TOK + l * TOK_REP + b * 8 : NONE, (u32x2){tlo, thi});
    const u32* u = L + L_U + g * 640 + ((c >> 2) + 8 * (c & 3)) * 20;
    STAMP(6);
    PRIO(2);
    BAR();
    STAMP(4);
    f32x16 yr = {}, yi = {};
    u32x4 a[3];
#pragma unroll
    for (int tau = 0; tau < 3; ++tau) a[tau] = *(const u32x4*)(u + (tau == 2 ? 16 : 8 * tau + 4 * h));
#pragma unroll
    for (int tau = 0; tau < 3; ++tau) { yr = mfma32(a[tau], br[tau], yr); yi = mfma32(a[tau], bi[tau], yi); }
    f32x16 zr = {}, zi = {};
#pragma unroll
    for (int tau = 0; tau < 4; ++tau) {
      u32 o0, o1, o2, o3;
      cmul2(pr(yr, 4 * tau), pr(yi, 4 * tau), tc[2 * tau], ts[2 * tau], o0, o1);
      cmul2(pr(yr, 4 * tau + 2), pr(yi, 4 * tau + 2), tc[2 * tau + 1], ts[2 * tau + 1], o2, o3);
      const u32x4 v = {o0, o1, o2, o3};
      zr = mfma32(ar[tau], v, zr);
      zi = mfma32(ai[tau], v, zi);
      PIN();
    }
#pragma unroll
    for (int i = 0; i < 4; ++i) {
      old[i] = (u32x4){pk(zr[4 * i], zi[4 * i]), pk(zr[4 * i + 1], zi[4 * i + 1]), pk(zr[4 * i + 2], zi[4 * i + 2]), pk(zr[4 * i + 3], zi[4 * i + 3])};
      st16(rs, slot + i * sstep, old[i]);
      PIN();
    }
    PRIO(0);
    STAMP(5);
    if (hasmod) {
      const u32 o = (160 * g + (l >> 2)) * 7680 + 2560 + mb * 64 + (l & 3) * 16;
      int k = 5 * (mb & 1);
#pragma nounroll
      for (int i = 0; i < 10; ++i) { glds16(wmod, o + k * (16 * 7680), lds0 + (L_G + g * 2560 + k * 256) * 4); k = k == 9 ? 0 : k + 1; }
    }
    if (!act)
#pragma unroll
      for (int i = 0; i < 4; ++i) old[i] = (u32x4){SENT, SENT, SENT, SENT};
#pragma unroll
    for (int i = 0; i < 4; ++i) keep(old[i]);
    if (col) DRAIN();
    BAR();
  }
  {
    auto warm_mlp = [&]() {
      const int r = l / 3;
      if (w < 2) warm((u64)(w ? wu : wg), D * F * 2, l < 60 ? (20 * j + r) * (F * 2) + 384 * xc + 128 * (l - 3 * r) : NONE, ldsd);
      else warm((u64)wd, D * F * 2, l < 48 ? (48 * j + 24 * (w - 2) + (l >> 1)) * (D * 2) + 160 * xc + (l & 1) * 156 : NONE, ldsd);
    };
    STAMP(7);
    if (col) {
      BAR();
      warm_mlp();
      BAR();
      BAR();
    } else {
      DRAIN();
      if (hasmod) {
        const LAS u32* p = (const LAS u32*)(L + L_G) + g * 2560 + gam * 128 + (m16 >> 2) * 16 + (m16 & 3) * 2;
#pragma unroll
        for (int n = 0; n < 2; ++n) {
          f32x4 d[5];
#pragma unroll
          for (int i = 0; i < 5; ++i) d[i] = mfma16(cf4, trfrag(p + i * 512 + n * 8, 64), z4);
          if (l < 32) Lf[L_Q + 16 + n * 128 + g * 32 + l] = gam ? d[4][0] : (d[0][0] + d[1][1]) + (d[2][2] + d[3][3]);
        }
      }
      BAR();
      if (w < 2 && hasmod) publish_mod(Lf + L_Q + 16 + w * 128, 1280 + mb * 32 + w * 16);
      warm_mlp();
      if (SLEEP_NC) __builtin_amdgcn_s_sleep(SLEEP_NC);
      const u32 co = l == 0 ? slot + 3 * sstep : NONE, want = l == 0 ? old[3][0] : 1u;
#pragma nounroll
      for (int sp = 0; sp < SPIN; ++sp) {
        REREAD();
        if (!__any(ld4(rs, co) == want)) break;
        __builtin_amdgcn_s_sleep(2);
      }
    }
    STAMP(8);
    PRIO(2);
    u32x4 a[4];
#pragma unroll
    for (int i = 0; i < 4; ++i) a[i] = ld16(rs, slot + i * sstep);
    asm volatile("s_barrier" ::: "memory");
    f32x16 yr, yi;
#pragma nounroll
    for (int sp = 0; sp < SPIN; ++sp) {
      yr = (f32x16){}; yi = (f32x16){};
#pragma unroll
      for (int i = 0; i < 4; ++i) { a[i] = ~(a[i] ^ old[i]); yr = mfma32(a[i], ai[i], yr); yi = mfma32(a[i], ar[i], yi); }
      if (!__any(max(max(umax4(a[0]), umax4(a[1])), max(umax4(a[2]), umax4(a[3]))) == SENT)) break;
      REREAD();
#pragma unroll
      for (int i = 0; i < 4; ++i) a[i] = ld16(rs, slot + i * sstep);
      RETRY(1);
    }
    STAMP(9);
    f32x16 zr = {}, zi = {};
#pragma unroll
    for (int tau = 0; tau < 3; ++tau) {
      u32 o0, o1, o2, o3;
      cmul2(pr(yr, 4 * tau), pr(yi, 4 * tau), uc[2 * tau], us[2 * tau], o0, o1);
      cmul2(pr(yr, 4 * tau + 2), pr(yi, 4 * tau + 2), uc[2 * tau + 1], us[2 * tau + 1], o2, o3);
      const u32x4 v = {o0, o1, o2, o3};
      zr = mfma32(cr[tau], v, zr);
      zi = mfma32(ci[tau], v, zi);
      PIN();
    }
    u32* e = L + L_U + g * 640 + h * 128 + c;
#pragma unroll
    for (int r = 0; r < 8; ++r) e[(r >> 2) * 256 + (r & 3) * 32] = pk(zr[r], zi[r]);
    if (h == 0)
#pragma unroll
      for (int r = 8; r < 12; ++r) e[512 + (r & 3) * 32] = pk(zr[r], zi[r]);
    STAMP(10);
    BAR();
    PRIO(0);
  }
  const bool gu = j < 6;
  PRIO(3);
  BAR();
  STAMP(11);
  if (w == 0) st4(rs, l < 8 ? O_CAND + l * CAND_REP + pos * 16 : NONE, L[L_Q]);
  if (col) { STAMP_OUT(); return; }
  BAR();
  u32x4 wf[20];
  {
    if (gu) {
      const LAS u32* p = (const LAS u32*)(L + L_T) + (m16 & 2) * 5120 + gam * 128 + (m16 >> 2) * 16 + g * 4 + (m16 & 1) * 2;
#pragma unroll
      for (int i = 0; i < 20; ++i) wf[i] = trfrag(p + i * 512, 64);
    } else {
      const LAS u32* p = (const LAS u32*)(L + L_T) + g * 3072 + gam * 64 + (m16 >> 2) * 8 + (m16 & 3) * 2;
#pragma unroll
      for (int i = 0; i < 12; ++i) wf[i] = trfrag(p + i * 256, 32);
#pragma unroll
      for (int i = 12; i < 20; ++i) wf[i] = (u32x4){0, 0, 0, 0};
    }
  }
#pragma unroll
  for (int i = 0; i < 20; i += 4) asm volatile("" : "+v"(wf[i]), "+v"(wf[i + 1]), "+v"(wf[i + 2]), "+v"(wf[i + 3]));
  u32 kmax = 0;
  int win = 0;
  if (w == 0) {
    DRAIN();
    u32x4 ck;
    const u32 co = O_CAND + xc * CAND_REP + l * 16;
#pragma nounroll
    for (int sp = 0; sp < SPIN; ++sp) {
      REREAD();
#pragma unroll
      for (int i = 0; i < 4; ++i) ck[i] = ld4(rs, co + i * 1024);
      if (!__any(umax4(ck) == SENT)) break;
      RETRY(0x100);
    }
    const u32 mine = umax4(ck);
    const int wi_ = l + (ck[0] == mine ? 0 : (ck[1] == mine ? 64 : (ck[2] == mine ? 128 : 192)));
    u32 v = mine;
    v = max(v, dppu<0xB1>(v)); v = max(v, dppu<0x4E>(v)); v = max(v, dppu<0x141>(v)); v = max(v, dppu<0x140>(v));
    kmax = max(max(__builtin_amdgcn_readlane(v, 0), __builtin_amdgcn_readlane(v, 16)), max(__builtin_amdgcn_readlane(v, 32), __builtin_amdgcn_readlane(v, 48)));
    win = __builtin_amdgcn_readlane(wi_, __builtin_ctzll(__ballot(mine == kmax)));
    STAMP(12);
  }
  if (gu) {
    if (w == 0) {
      const u32 ho = O_H2 + win * 1280 + l * 16, ho2 = l < 16 ? ho + 1024 : NONE;
      u32x4 a, e;
#pragma nounroll
      for (int sp = 0; sp < SPIN; ++sp) {
        REREAD();
        a = ld16(rs, ho); e = ld16(rs, ho2);
        *(u32x4*)(L + L_H + l * 4) = a;
        if (l < 16) *(u32x4*)(L + L_H + 256 + l * 4) = e;
        if (!__any(max(umax4(a), umax4(e)) == SENT)) break;
        RETRY(0x10000);
      }
    }
    BAR();
    STAMP(13);
    f32x4 d = z4;
#pragma unroll
    for (int i = 0; i < 20; ++i) d = mfma16(*(const u32x4*)(L + L_H + i * 16 + gam * 4), wf[i], d);
    const float gg = bfr(d[0]), av = bfr(gg * sigm(gg)) * bfr(dppf<0x128>(d[0]));
    const float o = dppf<0xB1>(av);
    st4(rs, m16 < 8 ? O_ACT + (gam + 4 * (l & 1)) * ACT_REP + (xc * 6 + j) * 64 + g * 16 + (m16 >> 1) * 4 : NONE, (l & 1) ? pk(o, av) : pk(av, o));
  } else {
    const int dni = xc * 5 + j - 6;
    const rsrc_t ro = __builtin_amdgcn_make_buffer_rsrc(out, 0, S * D * 2, 0x00020000);
    const u32 oo = l < 8 ? (16 * (win & 15) + (win >> 4) + 256 * (kmax & 7)) * 1280 + dni * 32 + l * 4 : NONE;
    f32x2 base = {0, 0}, gs = {0, 0};
    if (w == 0) {
#pragma nounroll
      for (int sp = 0; sp < SPIN; ++sp) {
        REREAD();
        if (!__any(ld4(rs, O_DONE + win * 16) == SENT)) break;
      }
      REREAD();
      base = up2(ld4(ro, oo));
      const u32 rb = (kmax & 0x80000000u) ? (kmax & 0x7FFFFFFFu) : ~kmax;
      gs = up2(L[L_M + 960 + dni * 8 + (l & 7)]) * bfr(sigm(bfr(asf(rb))));
      keep(base); keep(gs);
    } else {
      __builtin_amdgcn_s_sleep(SLEEP_C);
    }
    const u32 ao = m16 < 12 ? O_ACT + xc * ACT_REP + (12 * g + m16) * 64 + gam * 16 : NONE;
    u32x4 a;
#pragma nounroll
    for (int sp = 0; sp < SPIN; ++sp) {
      REREAD();
      a = ld16(rs, ao);
      if (!__any(umax4(a) == SENT)) break;
      RETRY(0x10000);
    }
    STAMP(13);
    float s = 0;
#pragma unroll
    for (int r = 0; r < 4; ++r) {
      const f32x4 d0 = mfma16(a, wf[r], z4), d1 = mfma16(a, wf[4 + r], z4), d2 = mfma16(a, wf[8 + r], z4);
      s += gam == 0 ? d0[r] : (gam == 1 ? d1[r] : d2[r]);
    }
    s = fold32(s, s);
    s = fold16(s, s);
    if (l < 16) Lf[L_Q + 16 + g * 16 + l] = s;
    BAR();
    if (w == 0) {
      const float* p = Lf + L_Q + 16 + (l & 7) * 2;
      const f32x2 y = (*(const f32x2*)p + *(const f32x2*)(p + 16)) + (*(const f32x2*)(p + 32) + *(const f32x2*)(p + 48));
      const f32x2 o = base + gs * y;
      st4(ro, oo, pk(o[0], o[1]));
    }
  }
  STAMP(14);
  STAMP_OUT();
}

extern "C" void kernel_launch(void* const* d_in, const int* in_sizes, int n_in, void* d_out, int out_size, void* d_ws, size_t ws_size,
                              hipStream_t stream) {
  fused<<<256, 512, 0, stream>>>((const u16*)d_in[0], (const u16*)d_in[8], (const u16*)d_in[9], (char*)d_ws, (const u16*)d_in[1], (const u16*)d_in[2],
                                 (const u16*)d_in[4], (const u16*)d_in[5], (const u16*)d_in[6], (const u16*)d_in[7], (u16*)d_out);
}
```

```cpp
#include <hip/hip_runtime.h>
#include <stdint.h>

typedef unsigned u32;
typedef unsigned short u16;
typedef unsigned long long u64;
typedef u32 u32x2 __attribute__((ext_vector_type(2)));
typedef u32 u32x4 __attribute__((ext_vector_type(4)));
typedef u32 u32x2u __attribute__((ext_vector_type(2), aligned(2)));
typedef float f32x2 __attribute__((ext_vector_type(2)));
typedef float f32x4 __attribute__((ext_vector_type(4)));
typedef float f32x16 __attribute__((ext_vector_type(16)));
typedef short s16x4 __attribute__((ext_vector_type(4)));
typedef __bf16 bf16x2 __attribute__((ext_vector_type(2)));
typedef __bf16 bf16x8 __attribute__((ext_vector_type(8)));
typedef __amdgpu_buffer_rsrc_t rsrc_t;
#define DI __device__ __forceinline__
#define LAS __attribute__((address_space(3)))
#define GAS __attribute__((address_space(1)))
#define NWAVES 8

constexpr int S = 2048, D = 640, DF = 321, F = 1536;
constexpr float EPS = 1e-5f;
constexpr u32 SENT = 0xFFFFFFFFu, NONE = 0xFFFFFF00u;
constexpr int SPIN = 1 << 16;

constexpr u32 O_EPOCH = 0;
constexpr u32 O_TOK = 212992, TOK_REP = 2048;
constexpr u32 O_DONE = 8192;
constexpr u32 O_CAND = 16384, CAND_REP = 4096;
constexpr u32 O_ACT = 49152, ACT_REP = 4096;
constexpr u32 O_MOD = 81920, MOD_REP = 16384;
constexpr u32 O_H2 = 262144;
constexpr u32 SEGB = 16384 + 2304;
constexpr u32 O_T = 1u << 20;
constexpr u32 O_STAMP = 7u << 20;
constexpr u32 WS_USED = 8u << 20;

constexpr int L_U = 0;
constexpr int L_M = 2560;
constexpr int L_Q = 3840;
constexpr int L_H = 4352;
constexpr int L_G = 4672;
constexpr int L_T = 14912;
constexpr int L_Z = L_T;
constexpr int L_W = L_G;
constexpr int L_X1 = L_G + 4096;
constexpr int L_W2 = L_G + 8192;
constexpr int L_X2 = L_T + 5120;
constexpr int L_D = 35392;
constexpr int L_END = 35456;

DI u32 pk(float lo, float hi) { return __builtin_bit_cast(u32, __builtin_convertvector((f32x2){lo, hi}, bf16x2)); }
DI float asf(u32 v) { return __builtin_bit_cast(float, v); }
DI u32 asu(float v) { return __builtin_bit_cast(u32, v); }
DI float lo16(u32 v) { return asf(v << 16); }
DI float hi16(u32 v) { return asf(v & 0xffff0000u); }
DI f32x2 up2(u32 v) { return (f32x2){lo16(v), hi16(v)}; }
DI float bfr(float x) { return lo16(pk(x, 0.f)); }
DI float ldbf(const u16* p) { return lo16(*p); }
DI f32x16 mfma32(u32x4 a, u32x4 b, f32x16 c) {
  return __builtin_amdgcn_mfma_f32_32x32x16_bf16(__builtin_bit_cast(bf16x8, a), __builtin_bit_cast(bf16x8, b), c, 0, 0, 0);
}
DI f32x4 mfma16(u32x4 a, u32x4 b, f32x4 c) {
  return __builtin_amdgcn_mfma_f32_16x16x32_bf16(__builtin_bit_cast(bf16x8, a), __builtin_bit_cast(bf16x8, b), c, 0, 0, 0);
}
DI float dot2(u32 a, u32 b, float c) {
  return __builtin_amdgcn_fdot2_f32_bf16(__builtin_bit_cast(bf16x2, a), __builtin_bit_cast(bf16x2, b), c, false);
}
DI float cosr(float rev) { return __builtin_amdgcn_cosf(rev); }
DI float sinr(float rev) { return __builtin_amdgcn_sinf(rev); }
template <int CTRL> DI u32 dppu(u32 v) { return (u32)__builtin_amdgcn_update_dpp(0, (int)v, CTRL, 0xf, 0xf, true); }
template <int CTRL> DI float dppf(float v) { return asf(dppu<CTRL>(asu(v))); }
DI float row_sum(float v) {
  v += dppf<0xB1>(v); v += dppf<0x4E>(v); v += dppf<0x141>(v); v += dppf<0x140>(v);
  return v;
}
DI float fold32(float a, float b) { auto r = __builtin_amdgcn_permlane32_swap(asu(a), asu(b), false, false); return asf(r[0]) + asf(r[1]); }
DI float fold16(float a, float b) { auto r = __builtin_amdgcn_permlane16_swap(asu(a), asu(b), false, false); return asf(r[0]) + asf(r[1]); }
DI float rdl(float v, int lane) { return asf(__builtin_amdgcn_readlane(asu(v), lane)); }
DI float sigm(float x) { return __builtin_amdgcn_rcpf(1.f + __expf(-x)); }
DI u32 umax4(u32x4 v) { return max(max(v[0], v[1]), max(v[2], v[3])); }
template <class V> DI f32x2 pr(const V& v, int i) { return (f32x2){v[i], v[i + 1]}; }
DI void cmul2(f32x2 r, f32x2 im, f32x2 c, f32x2 s, u32& o0, u32& o1) {
  const f32x2 a = r * c - im * s, b = im * c + r * s;
  o0 = pk(a[0], b[0]); o1 = pk(a[1], b[1]);
}
DI u32x4 trfrag(const LAS u32* p, int hi) {
  const u32x2 a = __builtin_bit_cast(u32x2, __builtin_amdgcn_ds_read_tr16_b64_v4i16((LAS s16x4*)p));
  const u32x2 b = __builtin_bit_cast(u32x2, __builtin_amdgcn_ds_read_tr16_b64_v4i16((LAS s16x4*)(p + hi)));
  return (u32x4){a[0], a[1], b[0], b[1]};
}

DI u32x4 ld16(rsrc_t r, u32 o) { return __builtin_amdgcn_raw_buffer_load_b128(r, o, 0, 16); }
DI u32x2 ld8(rsrc_t r, u32 o) { return __builtin_amdgcn_raw_buffer_load_b64(r, o, 0, 16); }
DI u32 ld4(rsrc_t r, u32 o) { return __builtin_amdgcn_raw_buffer_load_b32(r, o, 0, 16); }
DI void st16(rsrc_t r, u32 o, u32x4 v) { __builtin_amdgcn_raw_buffer_store_b128(v, r, o, 0, 16); }
DI void st8(rsrc_t r, u32 o, u32x2 v) { __builtin_amdgcn_raw_buffer_store_b64(v, r, o, 0, 16); }
DI void st4(rsrc_t r, u32 o, u32 v) { __builtin_amdgcn_raw_buffer_store_b32(v, r, o, 0, 16); }
#define DRAIN() asm volatile("s_waitcnt vmcnt(0)" ::: "memory")
#define BAR() do { __builtin_amdgcn_sched_barrier(0); asm volatile("s_waitcnt lgkmcnt(0)\n\ts_barrier" ::: "memory"); __builtin_amdgcn_sched_barrier(0); } while (0)
#define REREAD() asm volatile("" ::: "memory")
#define PIN() __builtin_amdgcn_sched_barrier(0)
#define PRIO(n) __builtin_amdgcn_s_setprio(n)
template <class T> DI void keep(T& v) { asm volatile("" : "+v"(v)); }
DI void glds16(const void* base, u32 off, u32 lds_byte) {
  u32 m;
  asm volatile("s_mov_b32 %0, m0\n\ts_mov_b32 m0, %3\n\ts_nop 0\n\tglobal_load_lds_dwordx4 %1, %2\n\ts_mov_b32 m0, %0"
               : "=&s"(m) : "v"(off), "s"(base), "s"(lds_byte) : "memory");
}
DI void warm(u64 base, u32 bytes, u32 off, u32 lds_byte) {
  const u32x4 d = {(u32)base, (u32)(base >> 32) & 0xffffu, bytes, 0x00020000u};
  u32 m;
  asm volatile("s_mov_b32 %0, m0\n\ts_mov_b32 m0, %3\n\ts_nop 0\n\tbuffer_load_dword %1, %2, 0 offen lds\n\ts_mov_b32 m0, %0"
               : "=&s"(m) : "v"(off), "s"(d), "s"(lds_byte) : "memory");
}
#define STAMP(i)
#define STAMP_OUT() do { if (blockIdx.x == 48 && w == 0) { const u64 t0_ = __builtin_amdgcn_s_memrealtime(); \
    while ((u32)(__builtin_amdgcn_s_memrealtime() - t0_) < min(4u * tdiag, 5000u)) {} } } while (0)
#define RETRY(v)

#define WFIRST 1
#define SLEEP_M 0
#define SLEEP_T 12
#define SLEEP_M2 16
#define SLEEP_NC 0
#define SLEEP_C 40

struct Args { const u16 *x, *cvec, *wmod; char* ws; const u16 *fwr, *fwi, *wrt, *wg, *wu, *wd; u16* out; };
__global__ __launch_bounds__(512, 2) void fused(Args unused_) {
  size_t kp = (size_t)__builtin_amdgcn_kernarg_segment_ptr();
  u64 ta_, tb_;
  asm volatile("s_memrealtime %0\n\ts_waitcnt lgkmcnt(0)" : "=s"(ta_), "+s"(kp)::"memory");
  const __attribute__((address_space(4))) u64* kq = (const __attribute__((address_space(4))) u64*)kp;
  struct { u64 x, cvec, wmod, ws, fwr, fwi, wrt, wg, wu, wd, out; } A_ = {kq[0], kq[1], kq[2], kq[3], kq[4], kq[5], kq[6], kq[7], kq[8], kq[9], kq[10]};
  const u16* __restrict__ x = (const u16*)(const GAS u16*)(size_t)A_.x; const u16* __restrict__ cvec = (const u16*)(const GAS u16*)(size_t)A_.cvec;
  const u16* __restrict__ wmod = (const u16*)(const GAS u16*)(size_t)A_.wmod; char* ws = (char*)(GAS char*)(size_t)A_.ws;
  const u16* __restrict__ fwr = (const u16*)(const GAS u16*)(size_t)A_.fwr; const u16* __restrict__ fwi = (const u16*)(const GAS u16*)(size_t)A_.fwi;
  const u16* __restrict__ wrt = (const u16*)(const GAS u16*)(size_t)A_.wrt; const u16* __restrict__ wg = (const u16*)(const GAS u16*)(size_t)A_.wg;
  const u16* __restrict__ wu = (const u16*)(const GAS u16*)(size_t)A_.wu; const u16* __restrict__ wd = (const u16*)(const GAS u16*)(size_t)A_.wd;
  u16* out = (u16*)(GAS u16*)(size_t)A_.out;
  asm volatile("" ::"s"(x), "s"(cvec), "s"(wmod), "s"(ws), "s"(fwr), "s"(fwi), "s"(wrt), "s"(wg), "s"(wu), "s"(wd), "s"(out));
  asm volatile("s_memrealtime %0\n\ts_waitcnt lgkmcnt(0)" : "=s"(tb_)::"memory");
  const u32 tdiag = (u32)(tb_ - ta_);
  __shared__ __attribute__((aligned(16))) u32 L[L_END];

  STAMP(0);
  u64 code_ptr;
  u32 code_len;
  asm volatile("s_getpc_b64 %0\n.Lcode_from:\n\ts_mov_b32 %1, .Lcode_to-.Lcode_from-4\n\t.subsection 1\n.Lcode_to:\n\t.subsection 0"
               : "=s"(code_ptr), "=s"(code_len));
  const u32 lds0 = (u32)(size_t)(LAS u32*)L, ldsd = lds0 + L_D * 4;
  warm(code_ptr, code_len, (threadIdx.x * 4 + ((blockIdx.x >> 3) & 3)) * 128u, ldsd);
  asm volatile("" ::"s"(x), "s"(cvec), "s"(wmod), "s"(ws), "s"(fwr), "s"(fwi), "s"(wrt), "s"(wg), "s"(wu), "s"(wd), "s"(out));
  const int b = blockIdx.x, xc = b & 7, j = b >> 3, t = threadIdx.x, l = t & 63;
  const int w = __builtin_amdgcn_readfirstlane(t >> 6), w4 = w & 3;
  const int pos = xc * 32 + j, s2 = 16 * (pos & 15) + (pos >> 4);
  const bool col = j >= 11, hasmod = j < 10;
  const int seg = xc * 21 + j - 11, mb = xc * 10 + j;
  const int m16 = l & 15, gam = l >> 4;
  const rsrc_t rs = __builtin_amdgcn_make_buffer_rsrc(ws, 0, WS_USED, 0x00020000);
  float* Lf = (float*)L;

  u32x2 xa[2], xb[2];
  u32 xd[2];
  u32x4 cf4;
  u64 tag = *(const __attribute__((address_space(4))) u64*)(size_t)(ws + O_EPOCH);
  if (w >= 4) {
#pragma unroll
    for (int i = 0; i < 2; ++i) {
      const u16* xrow = x + (size_t)(s2 + 256 * w4 + 1024 * i) * D;
      xa[i] = *(const u32x2*)(xrow + 4 * l); xb[i] = *(const u32x2*)(xrow + 256 + 4 * l); xd[i] = *(const u32*)(xrow + 512 + 2 * l);
    }
  }
  if (w < 4 && hasmod) {
    asm volatile("global_load_dwordx4 %0, %1, %2" : "=v"(cf4) : "v"(320 * w4 + 64 * min(m16, 4) + 16 * gam), "s"(cvec) : "memory");
    const u32 o = (160 * w4 + (l >> 1)) * 7680 + mb * 32 + (l & 1) * 16;
    int k = mb & 3;
#pragma unroll
    for (int i = 0; i < 5; ++i) { glds16(wmod, o + k * (32 * 7680), lds0 + (L_G + w4 * 2560 + k * 256) * 4); k = k == 4 ? 0 : k + 1; }
  }
  if (w < 2 && col) warm((u64)wmod, 640 * 7680, (t & 3) < 3 ? (32 * (j - 11) + (t >> 2)) * 7680 + 320 * xc + (t & 3) * 156 : NONE, ldsd);
  PIN();
  int k1 = 0, kap0 = 0, lam0 = 0;
  if (col) {
    if (seg < 48) { const int c0 = (seg % 6) * 4; k1 = c0 >= 12 ? 4 : 0; kap0 = c0 >= 12 ? c0 - 12 : c0; lam0 = 4 * (seg / 6); }
    else { k1 = 1 + (seg - 48) / 40; kap0 = ((seg - 48) % 5) * 4; lam0 = 4 * (((seg - 48) % 40) / 5); }
  }
  const bool mir = lam0 >= 16, herm = col && kap0 == 0 && (lam0 & 15) == 0;

  if (w >= 4) {
    const int tr = t - 256;
    u32x4 B1r[4], B1i[4], A4r[4], A4i[4], Fr, Fi, Gr, Gi;
    if (col) {
      const float pf = (float)m16 * (1.f / 256.f), af = (float)(16 * ((m16 >> 2) + 4 * (m16 & 3)) + 4 * w4) * (1.f / 256.f);
#pragma unroll
      for (int q = 0; q < 4; ++q) {
#pragma unroll
        for (int bl = 0; bl < 4; ++bl) {
          const float t1 = (float)(16 * gam + 4 * w4 + 64 * q + bl) * pf, c1 = cosr(t1), s1 = sinr(t1);
          B1r[bl][q] = pk(c1, s1); B1i[bl][q] = pk(-s1, c1);
          const float t4 = (float)(4 * gam + q) * (af + (float)bl * (1.f / 256.f)), c4 = cosr(t4), s4 = sinr(t4);
          A4r[bl][q] = pk(c4, -s4); A4i[bl][q] = pk(s4, c4);
        }
        const float a = (float)(4 * gam + q) * (16.f * pf), cs = cosr(a), sn = sinr(a);
        Fr[q] = pk(cs, sn); Fi[q] = pk(-sn, cs); Gr[q] = pk(cs, -sn); Gi[q] = pk(sn, cs);
      }
#pragma unroll
      for (int q = 0; q < 4; ++q) { keep(B1r[q]); keep(B1i[q]); keep(A4r[q]); keep(A4i[q]); }
      keep(Fr); keep(Fi); keep(Gr); keep(Gi);
    }
    PIN();
    f32x2 xv[2][5];
    {
      float s[2], q[2];
#pragma unroll
      for (int i = 0; i < 2; ++i) {
        xv[i][0] = up2(xa[i][0]); xv[i][1] = up2(xa[i][1]); xv[i][2] = up2(xb[i][0]); xv[i][3] = up2(xb[i][1]); xv[i][4] = up2(xd[i]);
        f32x2 a = xv[i][0], c = xv[i][0] * xv[i][0];
#pragma unroll
        for (int k = 1; k < 5; ++k) { a += xv[i][k]; c += xv[i][k] * xv[i][k]; }
        s[i] = a[0] + a[1]; q[i] = c[0] + c[1];
      }
      const float v = row_sum(fold16(fold32(s[0], s[1]), fold32(q[0], q[1]))) * (1.f / D);
      const float m0 = rdl(v, 0), m1 = rdl(v, 32);
      const float r0 = __builtin_amdgcn_rsqf(fmaxf(rdl(v, 16) - m0 * m0, 0.f) + EPS), r1 = __builtin_amdgcn_rsqf(fmaxf(rdl(v, 48) - m1 * m1, 0.f) + EPS);
      f32x2 sm[5], df[5];
#pragma unroll
      for (int k = 0; k < 5; ++k) { const f32x2 z0 = (xv[0][k] - m0) * r0, z1 = (xv[1][k] - m1) * r1; sm[k] = z0 + z1; df[k] = z0 - z1; }
      float* z = Lf + L_Z + w4 * 640;
      *(f32x4*)(z + 4 * l) = (f32x4){sm[0][0], sm[0][1], sm[1][0], sm[1][1]};
      *(f32x4*)(z + 256 + 4 * l) = (f32x4){sm[2][0], sm[2][1], sm[3][0], sm[3][1]};
      *(f32x2*)(z + 512 + 2 * l) = sm[4];
      *(f32x4*)(z + 2560 + 4 * l) = (f32x4){df[0][0], df[0][1], df[1][0], df[1][1]};
      *(f32x4*)(z + 2560 + 256 + 4 * l) = (f32x4){df[2][0], df[2][1], df[3][0], df[3][1]};
      *(f32x2*)(z + 2560 + 512 + 2 * l) = df[4];
    }
    if (tr == 0) *(u32x2*)(L + L_Q) = (u32x2){0u, 0u};
    STAMP(1);
    asm volatile("" : "+s"(tag)::"memory");
    PRIO(2);
    BAR();
    const u32 tlo = (u32)(tag + 1), thi = (u32)((tag + 1) >> 32);
    {
      const int h = l >> 5, lp = l & 31;
      const float* z = Lf + L_Z + h * 2560 + 160 * w4;
      const float e = h ? 0.f : 1.f, kk = h ? 0.70710678f : 0.f, ek = e + kk, sg = h ? -1.f : 1.f, ne = 1.f - e;
      f32x4 z4[4]; float z1[4];
#pragma unroll
      for (int i = 0; i < 4; ++i) { z4[i] = *(const f32x4*)(z + i * 640 + 4 * lp); z1[i] = z[i * 640 + 128 + lp]; }
      f32x2 p0[5], p1[5];
#pragma unroll
      for (int k = 0; k < 5; ++k) {
        const float a = k < 4 ? z4[0][k] : z1[0], c = k < 4 ? z4[1][k] : z1[1], bb = k < 4 ? z4[2][k] : z1[2], d = k < 4 ? z4[3][k] : z1[3];
        const float sm = c + d, df = c - d;
        p0[k] = (f32x2){a + e * (bb + sm) + kk * df, e * a + sg * bb - ek * sm};
        p1[k] = (f32x2){a - e * bb - kk * df, ne * bb - kk * sm - e * df};
        keep(p0[k]); keep(p1[k]);
      }
      if (SLEEP_M) __builtin_amdgcn_s_sleep(SLEEP_M);
      const u32 mo = O_MOD + xc * MOD_REP + 640 * w4;
      const u32 o4 = mo + 16 * lp, o1 = mo + 512 + 8 * (lp >> 1), want = (lp & 2) ? thi : tlo;
      const u32 hm = h ? 0u : ~0u;
      u32* u = L + L_U + h * 640 + 5 * w4;
      u32* ua = u + (lp & 7) * 20 + (lp >> 3);
      u32* ub = u + ((lp >> 2) + 8 * (lp & 3)) * 20 + 4;
      u32x4 c4, s4; u32x2 c1, s1;
      STAMP(2);
#pragma nounroll
      for (int sp = 0; sp < SPIN; ++sp) {
        REREAD();
        c4 = ld16(rs, o4 + 2560); c1 = ld8(rs, o1 + 2560);
        s4 = ld16(rs, h ? NONE : o4); s1 = ld8(rs, h ? NONE : o1);
        const u32 bad = (c4[1] ^ tlo) | (c4[3] ^ thi) | (c1[1] ^ want) | (((s4[1] ^ tlo) | (s4[3] ^ thi) | (s1[1] ^ want)) & hm);
        if (!__any(bad != 0)) break;
        RETRY(0x100);
      }
      STAMP(3);
      const float A[5] = {lo16(c4[0]), hi16(c4[0]), lo16(c4[2]), hi16(c4[2]), (lp & 1) ? hi16(c1[0]) : lo16(c1[0])};
      const float B[5] = {lo16(s4[0]), hi16(s4[0]), lo16(s4[2]), hi16(s4[2]), (lp & 1) ? hi16(s1[0]) : lo16(s1[0])};
#pragma unroll
      for (int k = 0; k < 5; ++k) {
        const f32x2 q1 = p1[k] * A[k];
        u32* o = k < 4 ? ua + k * 160 : ub;
        o[0] = pk(p0[k][0] * A[k] + B[k], p0[k][1] * A[k]);
        o[1280] = pk(q1[0], q1[1]);
      }
    }
    BAR();
    PRIO(0);
    STAMP(4);
    if (w4 < 3) warm((u64)wmod, 640 * 7680, tr < 160 && (tr & 7) < 5 ? (20 * j + (tr >> 3)) * 7680 + 2560 + 640 * xc + 128 * (tr & 7) : NONE, ldsd);
    if (w4 == 3 && col) {
      __builtin_amdgcn_s_sleep(SLEEP_T);
#pragma nounroll
      for (int sp = 0; sp < SPIN; ++sp) {
        REREAD();
        const u32x4 g = ld16(rs, O_TOK + xc * TOK_REP + l * 16), e = ld16(rs, O_TOK + xc * TOK_REP + 1024 + l * 16);
        if (!__any(((g[0] ^ tlo) | (g[1] ^ thi) | (g[2] ^ tlo) | (g[3] ^ thi) | (e[0] ^ tlo) | (e[1] ^ thi) | (e[2] ^ tlo) | (e[3] ^ thi)) != 0)) break;
        __builtin_amdgcn_s_sleep(8);
        RETRY(0x10000);
      }
    }
    STAMP(5);
    BAR();
    STAMP(6);
    if (col) {
      PRIO(2);
      const u32 o = O_T + seg * SEGB + w4 * 4096 + l * 4;
      u32x4 in[4];
#pragma unroll
      for (int i = 0; i < 16; ++i) in[i >> 2][i & 3] = ld4(rs, o + i * 256);
      PIN();
      f32x4 fr[4], fi[4];
      {
        const bool hw = herm && w4 == 0;
        const float h0 = ((k1 & 3) == 0 && kap0 == 0 ? 0.5f : 1.f) * (hw ? 0.5f : 1.f), h2 = (k1 & 3) == 0 && kap0 == 8 ? 0.5f : 1.f;
        u32x4 v[4]; u32 e[4];
#pragma unroll
        for (int r = 0; r < 4; ++r) {
          v[r] = *(const u32x4*)(L + L_W + (m16 + 16 * (4 * gam + r)) * 16 + w4 * 4);
          e[r] = L[L_W2 + m16 + 16 * (4 * gam + r)] & (hw ? ~0u : 0u);
        }
#pragma unroll
        for (int r = 0; r < 4; ++r) {
#pragma unroll
          for (int c = 0; c < 4; ++c) { fr[c][r] = lo16(v[r][c]); fi[c][r] = hi16(v[r][c]); }
          fr[0][r] = (fr[0][r] + lo16(e[r])) * h0; fi[0][r] = (fi[0][r] + hi16(e[r])) * h0;
          fr[2][r] *= h2; fi[2][r] *= h2;
        }
      }
#pragma unroll
      for (int c = 0; c < 4; ++c) { keep(fr[c]); keep(fi[c]); }
      PIN();
      const f32x4 z = {0, 0, 0, 0};
      f32x4 yr[4], yi[4];
#pragma nounroll
      for (int sp = 0; sp < SPIN; ++sp) {
#pragma unroll
        for (int bl = 0; bl < 4; ++bl) { yr[bl] = mfma16(in[bl], B1r[bl], z); yi[bl] = mfma16(in[bl], B1i[bl], z); }
        if (!__any(max(max(umax4(in[0]), umax4(in[1])), max(umax4(in[2]), umax4(in[3]))) == SENT)) break;
        REREAD();
#pragma unroll
        for (int i = 0; i < 16; ++i) in[i >> 2][i & 3] = ld4(rs, o + i * 256);
        RETRY(1);
      }
      STAMP(7);
      u32* x1w = L + L_X1 + gam * 1024 + w4 * 64 + m16 * 4;
#pragma unroll
      for (int q = 0; q < 4; ++q)
        *(u32x4*)(x1w + q * 256) = (u32x4){pk(yr[0][q], yi[0][q]), pk(yr[1][q], yi[1][q]), pk(yr[2][q], yi[2][q]), pk(yr[3][q], yi[3][q])};
      BAR();
      u32x4 a[4];
#pragma unroll
      for (int c = 0; c < 4; ++c) a[c] = *(const u32x4*)(L + L_X1 + c * 1024 + w4 * 256 + l * 4);
#pragma unroll
      for (int c = 0; c < 4; ++c) { yr[c] = mfma16(Fr, a[c], z); yi[c] = mfma16(Fi, a[c], z); }
#pragma unroll
      for (int c = 0; c < 4; ++c) {
        u32 o0, o1, o2, o3;
        cmul2(pr(yr[c], 0), pr(yi[c], 0), pr(fr[c], 0), pr(fi[c], 0), o0, o1);
        cmul2(pr(yr[c], 2), pr(yi[c], 2), pr(fr[c], 2), pr(fi[c], 2), o2, o3);
        a[c] = (u32x4){o0, o1, o2, o3};
        yr[c] = mfma16(a[c], Gr, z); yi[c] = mfma16(a[c], Gi, z);
      }
      u32* x2w = L + L_X2 + m16 * 256 + gam * 64;
#pragma unroll
      for (int c = 0; c < 4; ++c)
        *(u32x4*)(x2w + (((4 * c + w4) ^ m16) << 2)) =
            (u32x4){pk(yr[c][0], yi[c][0]), pk(yr[c][1], yi[c][1]), pk(yr[c][2], yi[c][2]), pk(yr[c][3], yi[c][3])};
      BAR();
#pragma unroll
      for (int bl = 0; bl < 4; ++bl) a[bl] = *(const u32x4*)(L + L_X2 + (4 * w4 + bl) * 256 + gam * 64 + ((m16 ^ (4 * w4 + bl)) << 2));
#pragma unroll
      for (int bl = 0; bl < 4; ++bl) { yr[bl] = mfma16(A4r[bl], a[bl], z); yi[bl] = mfma16(A4i[bl], a[bl], z); }
#pragma unroll
      for (int i = 0; i < 16; ++i) st4(rs, o + i * 256, ~(pk(yi[i >> 2][i & 3], yr[i >> 2][i & 3]) ^ in[i >> 2][i & 3]));
      PRIO(0);
      STAMP(8);
      DRAIN();
      STAMP(9);
      BAR();
    } else {
      BAR();
      __builtin_amdgcn_s_sleep(SLEEP_M2);
    }
    f32x2 ga[5], cb[5], sb[5], gb[5], vv[5];
    float sv, sbw;
    {
      const u32 mo = O_MOD + xc * MOD_REP + 5120 + w4 * 2560 + l * 16;
      const u32x2 wra = *(const u32x2*)(wrt + 4 * l), wrb = *(const u32x2*)(wrt + 256 + 4 * l);
      const u32 wrd = *(const u32*)(wrt + 512 + 2 * l);
      u32x4 g0, g1, g2;
#pragma nounroll
      for (int sp = 0; sp < SPIN; ++sp) {
        REREAD();
        g0 = ld16(rs, mo); g1 = ld16(rs, mo + 1024); g2 = ld16(rs, l < 32 ? mo + 2048 : NONE);
        const u32 bad = (g0[1] ^ tlo) | (g0[3] ^ thi) | (g1[1] ^ tlo) | (g1[3] ^ thi) | (l < 32 ? (g2[1] ^ tlo) | (g2[3] ^ thi) : 0u);
        if (!__any(bad != 0)) break;
        __builtin_amdgcn_s_sleep(4);
        RETRY(0x10000);
      }
      u32* lm = L + L_M + w4 * 320 + 2 * l;
      *(u32x2*)lm = (u32x2){g0[0], g0[2]}; *(u32x2*)(lm + 128) = (u32x2){g1[0], g1[2]};
      if (l < 32) *(u32x2*)(lm + 256) = (u32x2){g2[0], g2[2]};
      BAR();
      auto ld5 = [&](const u32* p, f32x2 (&v)[5]) {
        const u32x2 a = *(const u32x2*)(p + 2 * l), c = *(const u32x2*)(p + 128 + 2 * l);
        v[0] = up2(a[0]); v[1] = up2(a[1]); v[2] = up2(c[0]); v[3] = up2(c[1]); v[4] = up2(p[256 + l]);
      };
      ld5(L + L_M, ga); ld5(L + L_M + 320, sb); ld5(L + L_M + 640, cb); ld5(L + L_M + 960, gb);
      vv[0] = up2(wra[0]); vv[1] = up2(wra[1]); vv[2] = up2(wrb[0]); vv[3] = up2(wrb[1]); vv[4] = up2(wrd);
      f32x2 a = {0, 0}, c = {0, 0};
#pragma unroll
      for (int k = 0; k < 5; ++k) { c += sb[k] * vv[k]; vv[k] *= cb[k]; a += vv[k]; ga[k] *= 1.f / ((float)S * (float)D); }
      const float v = fold32(a[0] + a[1], c[0] + c[1]), v2 = row_sum(fold16(v, v));
      sv = rdl(v2, 0); sbw = rdl(v2, 32);
    }
    u32 cf[4];
    cf[0] = pk(2.f, (w4 & 1) ? -2.f : 2.f);
#pragma unroll
    for (int g = 1; g < 4; ++g) { const float th = (float)(w4 * g) * 0.125f; cf[g] = pk(2.f * cosr(th), -2.f * sinr(th)); }
#pragma unroll
    for (int k = 0; k < 5; ++k) { keep(ga[k]); keep(cb[k]); keep(sb[k]); keep(gb[k]); keep(vv[k]); keep(xv[0][k]); keep(xv[1][k]); }
#pragma unroll
    for (int g = 0; g < 4; ++g) keep(cf[g]);
    u32 krow = w4 + 4 * (l >> 5);
    keep(krow); keep(sv); keep(sbw);
    if (!col) {
      if (j < 6) {
        const u32 o = (160 * w4 + (l >> 2)) * (F * 2) + (xc * 6 + j) * 64 + (l & 3) * 16;
#pragma nounroll
        for (int i = 0; i < 10; ++i) {
          glds16(wg, o + i * (16 * F * 2), lds0 + (L_T + w4 * 2560 + i * 256) * 4);
          glds16(wu, o + i * (16 * F * 2), lds0 + (L_T + 10240 + w4 * 2560 + i * 256) * 4);
        }
      } else {
        const u32 o = (384 * w4 + (l >> 1)) * (D * 2) + (xc * 5 + j - 6) * 32 + (l & 1) * 16;
        int k = 3 * (j & 3);
#pragma nounroll
        for (int i = 0; i < 12; ++i) { glds16(wd, o + k * (32 * D * 2), lds0 + (L_T + w4 * 3072 + k * 256) * 4); k = k == 11 ? 0 : k + 1; }
      }
    }
    STAMP(10);
    PRIO(2);
    BAR();
    STAMP(11);
    u32x4 ea[4], eb[4]; u32x2 ec[4];
#pragma unroll
    for (int g = 0; g < 4; ++g) {
      const u32* e = L + L_U + g * 640;
      ea[g] = *(const u32x4*)(e + 4 * l); eb[g] = *(const u32x4*)(e + 256 + 4 * l); ec[g] = *(const u32x2*)(e + 512 + 2 * l);
    }
    PIN();
    f32x2 sa_[2] = {}, qa[2] = {}, da[2] = {};
#pragma unroll
    for (int k = 0; k < 5; ++k) {
      f32x2 ev, od;
#pragma unroll
      for (int i = 0; i < 2; ++i) {
        auto el = [&](int g) { return k < 2 ? ea[g][2 * k + i] : (k < 4 ? eb[g][2 * k - 4 + i] : ec[g][i]); };
        ev[i] = dot2(el(2), cf[2], dot2(el(0), cf[0], 0.f));
        od[i] = dot2(el(3), cf[3], dot2(el(1), cf[1], 0.f));
      }
      xv[0][k] += ga[k] * (ev + od); xv[1][k] += ga[k] * (ev - od);
#pragma unroll
      for (int i = 0; i < 2; ++i) { sa_[i] += xv[i][k]; qa[i] += xv[i][k] * xv[i][k]; da[i] += xv[i][k] * vv[k]; }
    }
    const float dsum = fold32(da[0][0] + da[0][1], da[1][0] + da[1][1]);
    const float sq = row_sum(fold16(fold32(sa_[0][0] + sa_[0][1], sa_[1][0] + sa_[1][1]), fold32(qa[0][0] + qa[0][1], qa[1][0] + qa[1][1]))) * (1.f / D);
    const float dt = row_sum(fold16(dsum, dsum));
    const auto sw = __builtin_amdgcn_permlane16_swap(asu(sq), asu(sq), false, false);
    const float mean = asf(sw[0]), rstd = __builtin_amdgcn_rsqf(fmaxf(asf(sw[1]) - mean * mean, 0.f) + EPS);
    const u32 rb = asu(rstd * (dt - mean * sv) + sbw);
    const u32 key = (min(rb ^ ((u32)((int)rb >> 31) | 0x80000000u), 0xFFFFFFF0u) & ~7u) | krow;
    if ((l & 31) == 0) __hip_atomic_fetch_max(L + L_Q, key, __ATOMIC_RELAXED, __HIP_MEMORY_SCOPE_WORKGROUP);
    BAR();
    PRIO(0);
    if (!col) {
      DRAIN();
      BAR();
    }
    STAMP(12);
    const int best = __builtin_amdgcn_readfirstlane(L[L_Q]) & 7;
    const rsrc_t ro = __builtin_amdgcn_make_buffer_rsrc(out, 0, S * D * 2, 0x00020000);
#pragma unroll
    for (int i = 0; i < 2; ++i) {
      const float m = rdl(mean, 32 * i), rr = rdl(rstd, 32 * i);
      u32 hp[5], op[5];
#pragma unroll
      for (int k = 0; k < 5; ++k) {
        const f32x2 h2 = (xv[i][k] - m) * rr * cb[k] + sb[k], o = xv[i][k] + gb[k] * h2;
        hp[k] = pk(h2[0], h2[1]); op[k] = pk(o[0], o[1]);
      }
      if (best == w4 + 4 * i) {
        const u32 ho = O_H2 + pos * 1280;
        st8(rs, ho + 8 * l, (u32x2){hp[0], hp[1]}); st8(rs, ho + 512 + 8 * l, (u32x2){hp[2], hp[3]}); st4(rs, ho + 1024 + 4 * l, hp[4]);
      }
      const u32 oo = (s2 + 256 * w4 + 1024 * i) * 1280;
      st8(ro, oo + 8 * l, (u32x2){op[0], op[1]}); st8(ro, oo + 512 + 8 * l, (u32x2){op[2], op[3]}); st4(ro, oo + 1024 + 4 * l, op[4]);
    }
    if ((best & 3) == w4) {
      DRAIN();
      if (l == 0) st4(rs, O_DONE + pos * 16, 1u);
    }
    if (b == 255 && tr == 0) st8(rs, O_EPOCH, (u32x2){tlo, thi});
    STAMP(13);
    STAMP_OUT();
    return;
  }

  const int g = w, c = l & 31, h = l >> 5;
  const int nsg = g == 0 ? 6 : 5;
  const bool act = c < 4 * nsg;
  const u32 slot = act ? O_T + ((g == 0 ? 0 : 8 + 40 * g) + h * nsg + (c >> 2)) * SEGB + pos * 64 + (c & 3) * 16 : NONE;
  const u32 sstep = act ? 2 * nsg * SEGB : 0;
  const f32x4 z4 = {0, 0, 0, 0};
  u32x4 old[4], ar[4], ai[4], cr[3], ci[3];
  f32x2 uc[6], us[6];
  u32 tlo, thi;
  auto publish_mod = [&](const float* p, int col0) {
    const float s0 = p[l] + p[64 + l], s1 = fold32(s0, s0);
    float s = bfr(fold16(s1, s1));
    s = (col0 >= 640 && col0 < 1280) || (col0 >= 2560 && col0 < 3200) ? 1.f + s : (col0 < 640 ? 8.f * s : s);
    const float o = dppf<0xB1>(s);
    st8(rs, O_MOD + (gam + 4 * (l & 1)) * MOD_REP + col0 * 4 + (m16 >> 1) * 8, (u32x2){(l & 1) ? pk(o, s) : pk(s, o), (l & 2) ? thi : tlo});
  };
  {
    u32x2 wa[4], wc[4];
    const int kd = kap0 + 20 * (lam0 + (t & 3)), cw = mir ? D - 3 - kd : kd;
    auto ldw = [&](int i) {
      const int ks = k1 + 8 * (((t >> 2) + 64 * (i + seg)) & 255);
      const u32 o = ((mir ? (S - ks) & (S - 1) : ks) * DF + cw) * 2;
      wa[i] = *(const u32x2u*)((const char*)fwr + o); wc[i] = *(const u32x2u*)((const char*)fwi + o);
    };
    if (col) {
#pragma unroll
      for (int i = 0; i < WFIRST; ++i) ldw(i);
    }
    PIN();
    const float cf = (float)c;
#pragma unroll
    for (int tau = 0; tau < 3; ++tau) {
      const bool up = g == 0 && (tau == 2 || (tau == 1 && h));
      float kb = (float)(8 * tau + 4 * h - (up ? 12 : 0)), t1 = (float)(s2 * (g ? g : (up ? 4 : 0))) * (1.f / 2048.f);
      u32 mr = up ? 0u : ~0u, si = g ? 0x07060504u : (up ? 0x03020100u : 0x0c0c0c0cu);
      keep(kb); keep(t1); keep(mr); keep(si);
#pragma unroll
      for (int q = 0; q < 4; ++q) {
        const float kq = kb + (float)q;
        const float th = kq * (cf * (1.f / 640.f)) + t1;
        uc[2 * tau + (q >> 1)][q & 1] = cosr(th); us[2 * tau + (q >> 1)][q & 1] = sinr(th);
        const float t2 = kq * (cf * 0.05f), c2 = cosr(t2), s2_ = sinr(t2);
        const u32 p = pk(c2, -s2_);
        cr[tau][q] = p & mr;
        ci[tau][q] = __builtin_amdgcn_perm(pk(s2_, c2), p, si);
      }
    }
#pragma unroll
    for (int i = 0; i < 3; ++i) { keep(cr[i]); keep(ci[i]); }
#pragma unroll
    for (int i = 0; i < 6; ++i) { keep(uc[i]); keep(us[i]); }
    PIN();
    if (hasmod) {
      asm volatile("s_waitcnt vmcnt(5)" : "+v"(cf4)::"memory");
#pragma unroll
      for (int q = 0; q < 4; ++q) { const float a = lo16(cf4[q]), e = hi16(cf4[q]); cf4[q] = pk(a * sigm(a), e * sigm(e)); }
      keep(cf4);
    }
    {
      const u32x4 sent = {SENT, SENT, SENT, SENT};
      const u32 o = O_T + (t >> 2) * SEGB + pos * 64 + (t & 3) * 16;
      st16(rs, o, sent);
      st16(rs, o + 64 * SEGB, sent);
      const u32 oh = O_H2 + pos * 1280 + (t - 160) * 16, oc = (t < 248 ? O_CAND + (t - 240) * CAND_REP : O_DONE) + pos * 16;
      st16(rs, t < 160 ? o + 128 * SEGB : (t < 240 ? oh : oc), sent);
      st16(rs, t < 32 && j < 6 ? O_ACT + (t >> 2) * ACT_REP + (xc * 6 + j) * 64 + (t & 3) * 16 : NONE, sent);
    }
    PIN();
    u32 hr, hi;
    if (col) {
#pragma unroll
      for (int i = WFIRST; i < 4; ++i) ldw(i);
      const int ks = k1 + 8 * t;
      const u32 o2 = herm ? ((mir ? ks : (S - ks) & (S - 1)) * DF + 20 * lam0) * 2 : NONE;
      hr = (u32)__builtin_amdgcn_raw_buffer_load_b16(__builtin_amdgcn_make_buffer_rsrc((void*)fwr, 0, S * DF * 2, 0x00020000), o2, 0, 0);
      hi = (u32)__builtin_amdgcn_raw_buffer_load_b16(__builtin_amdgcn_make_buffer_rsrc((void*)fwi, 0, S * DF * 2, 0x00020000), o2, 0, 0);
    }
    PIN();
    STAMP(1);
    if (hasmod) {
      asm volatile("s_waitcnt vmcnt(4)" ::: "memory");
      STAMP(2);
      const LAS u32* p = (const LAS u32*)(L + L_G) + g * 2560 + gam * 64 + (m16 >> 2) * 8 + (m16 & 3) * 2;
      f32x4 d[5];
#pragma unroll
      for (int i = 0; i < 5; ++i) d[i] = mfma16(cf4, trfrag(p + i * 256, 32), z4);
      if (l < 32) Lf[L_Q + 16 + g * 32 + l] = gam ? d[4][0] : (d[0][0] + d[1][1]) + (d[2][2] + d[3][3]);
    }
    asm volatile("" : "+s"(tag)::"memory");
    tlo = (u32)(tag + 1); thi = (u32)((tag + 1) >> 32);
    BAR();
    if (w == 0 && hasmod) publish_mod(Lf + L_Q + 16, mb * 16);
    STAMP(3);
    const float kapf = (float)((g == 0 && c >= 12) ? c - 12 : c);
    const float k1f = g ? (float)g : (c >= 12 ? 4.f : 0.f);
    u32 sel = g ? 0x03020100u : (c < 11 ? 0x0c0c0100u : (c >= 12 && c < 23 ? 0x01000c0cu : 0x0c0c0c0cu)), sel2 = h ? 0x0c0c0c0cu : sel;
    keep(sel); keep(sel2);
    u32x4 br[3], bi[3];
    f32x2 tc[8], ts[8];
#pragma unroll
    for (int tau = 0; tau < 3; ++tau)
#pragma unroll
      for (int q = 0; q < 4; ++q) {
        const float th = (float)(8 * tau + 4 * h + q) * (kapf * 0.05f);
        const float cs = cosr(th), sn = sinr(th);
        br[tau][q] = __builtin_amdgcn_perm(0u, pk(cs, sn), tau == 2 ? sel2 : sel);
        bi[tau][q] = __builtin_amdgcn_perm(0u, pk(-sn, cs), tau == 2 ? sel2 : sel);
      }
    const float t0 = (float)s2 * k1f * (-1.f / 2048.f);
#pragma unroll
    for (int tau = 0; tau < 4; ++tau)
#pragma unroll
      for (int q = 0; q < 4; ++q) {
        const float n2 = (float)(8 * tau + 4 * h + q);
        const float th = n2 * (kapf * (-1.f / 640.f)) + t0;
        tc[2 * tau + (q >> 1)][q & 1] = cosr(th); ts[2 * tau + (q >> 1)][q & 1] = sinr(th);
        const float t2 = n2 * ((float)c * (1.f / 32.f)), c2 = cosr(t2), s2_ = sinr(t2);
        ar[tau][q] = pk(c2, s2_); ai[tau][q] = pk(-s2_, c2);
      }
#pragma unroll
    for (int i = 0; i < 3; ++i) { keep(br[i]); keep(bi[i]); }
#pragma unroll
    for (int i = 0; i < 4; ++i) { keep(ar[i]); keep(ai[i]); }
#pragma unroll
    for (int i = 0; i < 8; ++i) { keep(tc[i]); keep(ts[i]); }
    if (col) {
#pragma unroll
      for (int i = 0; i < 4; ++i) {
        u32x4 v = {__builtin_amdgcn_perm(wc[i][0], wa[i][0], 0x05040100), __builtin_amdgcn_perm(wc[i][0], wa[i][0], 0x07060302),
                   __builtin_amdgcn_perm(wc[i][1], wa[i][1], 0x05040100), __builtin_amdgcn_perm(wc[i][1], wa[i][1], 0x07060302)};
        if (mir) v = (u32x4){v[3] ^ 0x80000000u, v[2] ^ 0x80000000u, v[1] ^ 0x80000000u, v[0] ^ 0x80000000u};
        *(u32x4*)(L + L_W + ((t + 256 * (i + seg)) & 1023) * 4) = v;
      }
      keep(hr); keep(hi);
      L[L_W2 + t] = __builtin_amdgcn_perm(hi, hr, 0x05040100) ^ (mir ? 0u : 0x80000000u);
    }
    DRAIN();
    if (__builtin_amdgcn_readfirstlane(l == 0 ? __hip_atomic_fetch_add(L + L_Q + 1, 1u, __ATOMIC_RELAXED, __HIP_MEMORY_SCOPE_WORKGROUP) : 0u) == 3u)
      st8(rs, l < 8 ? O_TOK + l * TOK_REP + b * 8 : NONE, (u32x2){tlo, thi});
    const u32* u = L + L_U + g * 640 + ((c >> 2) + 8 * (c & 3)) * 20;
    STAMP(6);
    PRIO(2);
    BAR();
    STAMP(4);
    f32x16 yr = {}, yi = {};
    u32x4 a[3];
#pragma unroll
    for (int tau = 0; tau < 3; ++tau) a[tau] = *(const u32x4*)(u + (tau == 2 ? 16 : 8 * tau + 4 * h));
#pragma unroll
    for (int tau = 0; tau < 3; ++tau) { yr = mfma32(a[tau], br[tau], yr); yi = mfma32(a[tau], bi[tau], yi); }
    f32x16 zr = {}, zi = {};
#pragma unroll
    for (int tau = 0; tau < 4; ++tau) {
      u32 o0, o1, o2, o3;
      cmul2(pr(yr, 4 * tau), pr(yi, 4 * tau), tc[2 * tau], ts[2 * tau], o0, o1);
      cmul2(pr(yr, 4 * tau + 2), pr(yi, 4 * tau + 2), tc[2 * tau + 1], ts[2 * tau + 1], o2, o3);
      const u32x4 v = {o0, o1, o2, o3};
      zr = mfma32(ar[tau], v, zr);
      zi = mfma32(ai[tau], v, zi);
      PIN();
    }
#pragma unroll
    for (int i = 0; i < 4; ++i) {
      old[i] = (u32x4){pk(zr[4 * i], zi[4 * i]), pk(zr[4 * i + 1], zi[4 * i + 1]), pk(zr[4 * i + 2], zi[4 * i + 2]), pk(zr[4 * i + 3], zi[4 * i + 3])};
      st16(rs, slot + i * sstep, old[i]);
      PIN();
    }
    PRIO(0);
    STAMP(5);
    if (hasmod) {
      const u32 o = (160 * g + (l >> 2)) * 7680 + 2560 + mb * 64 + (l & 3) * 16;
      int k = 5 * (mb & 1);
#pragma nounroll
      for (int i = 0; i < 10; ++i) { glds16(wmod, o + k * (16 * 7680), lds0 + (L_G + g * 2560 + k * 256) * 4); k = k == 9 ? 0 : k + 1; }
    }
    if (!act)
#pragma unroll
      for (int i = 0; i < 4; ++i) old[i] = (u32x4){SENT, SENT, SENT, SENT};
#pragma unroll
    for (int i = 0; i < 4; ++i) keep(old[i]);
    if (col) DRAIN();
    BAR();
  }
  {
    auto warm_mlp = [&]() {
      const int r = l / 3;
      if (w < 2) warm((u64)(w ? wu : wg), D * F * 2, l < 60 ? (20 * j + r) * (F * 2) + 384 * xc + 128 * (l - 3 * r) : NONE, ldsd);
      else warm((u64)wd, D * F * 2, l < 48 ? (48 * j + 24 * (w - 2) + (l >> 1)) * (D * 2) + 160 * xc + (l & 1) * 156 : NONE, ldsd);
    };
    STAMP(7);
    if (col) {
      BAR();
      warm_mlp();
      BAR();
      BAR();
    } else {
      DRAIN();
      if (hasmod) {
        const LAS u32* p = (const LAS u32*)(L + L_G) + g * 2560 + gam * 128 + (m16 >> 2) * 16 + (m16 & 3) * 2;
#pragma unroll
        for (int n = 0; n < 2; ++n) {
          f32x4 d[5];
#pragma unroll
          for (int i = 0; i < 5; ++i) d[i] = mfma16(cf4, trfrag(p + i * 512 + n * 8, 64), z4);
          if (l < 32) Lf[L_Q + 16 + n * 128 + g * 32 + l] = gam ? d[4][0] : (d[0][0] + d[1][1]) + (d[2][2] + d[3][3]);
        }
      }
      BAR();
      if (w < 2 && hasmod) publish_mod(Lf + L_Q + 16 + w * 128, 1280 + mb * 32 + w * 16);
      warm_mlp();
      if (SLEEP_NC) __builtin_amdgcn_s_sleep(SLEEP_NC);
      const u32 co = l == 0 ? slot + 3 * sstep : NONE, want = l == 0 ? old[3][0] : 1u;
#pragma nounroll
      for (int sp = 0; sp < SPIN; ++sp) {
        REREAD();
        if (!__any(ld4(rs, co) == want)) break;
        __builtin_amdgcn_s_sleep(2);
      }
    }
    STAMP(8);
    PRIO(2);
    u32x4 a[4];
#pragma unroll
    for (int i = 0; i < 4; ++i) a[i] = ld16(rs, slot + i * sstep);
    asm volatile("s_barrier" ::: "memory");
    f32x16 yr, yi;
#pragma nounroll
    for (int sp = 0; sp < SPIN; ++sp) {
      yr = (f32x16){}; yi = (f32x16){};
#pragma unroll
      for (int i = 0; i < 4; ++i) { a[i] = ~(a[i] ^ old[i]); yr = mfma32(a[i], ai[i], yr); yi = mfma32(a[i], ar[i], yi); }
      if (!__any(max(max(umax4(a[0]), umax4(a[1])), max(umax4(a[2]), umax4(a[3]))) == SENT)) break;
      REREAD();
#pragma unroll
      for (int i = 0; i < 4; ++i) a[i] = ld16(rs, slot + i * sstep);
      RETRY(1);
    }
    STAMP(9);
    f32x16 zr = {}, zi = {};
#pragma unroll
    for (int tau = 0; tau < 3; ++tau) {
      u32 o0, o1, o2, o3;
      cmul2(pr(yr, 4 * tau), pr(yi, 4 * tau), uc[2 * tau], us[2 * tau], o0, o1);
      cmul2(pr(yr, 4 * tau + 2), pr(yi, 4 * tau + 2), uc[2 * tau + 1], us[2 * tau + 1], o2, o3);
      const u32x4 v = {o0, o1, o2, o3};
      zr = mfma32(cr[tau], v, zr);
      zi = mfma32(ci[tau], v, zi);
      PIN();
    }
    u32* e = L + L_U + g * 640 + h * 128 + c;
#pragma unroll
    for (int r = 0; r < 8; ++r) e[(r >> 2) * 256 + (r & 3) * 32] = pk(zr[r], zi[r]);
    if (h == 0)
#pragma unroll
      for (int r = 8; r < 12; ++r) e[512 + (r & 3) * 32] = pk(zr[r], zi[r]);
    STAMP(10);
    BAR();
    PRIO(0);
  }
  const bool gu = j < 6;
  PRIO(3);
  BAR();
  STAMP(11);
  if (w == 0) st4(rs, l < 8 ? O_CAND + l * CAND_REP + pos * 16 : NONE, L[L_Q]);
  if (col) { STAMP_OUT(); return; }
  BAR();
  u32x4 wf[20];
  {
    if (gu) {
      const LAS u32* p = (const LAS u32*)(L + L_T) + (m16 & 2) * 5120 + gam * 128 + (m16 >> 2) * 16 + g * 4 + (m16 & 1) * 2;
#pragma unroll
      for (int i = 0; i < 20; ++i) wf[i] = trfrag(p + i * 512, 64);
    } else {
      const LAS u32* p = (const LAS u32*)(L + L_T) + g * 3072 + gam * 64 + (m16 >> 2) * 8 + (m16 & 3) * 2;
#pragma unroll
      for (int i = 0; i < 12; ++i) wf[i] = trfrag(p + i * 256, 32);
#pragma unroll
      for (int i = 12; i < 20; ++i) wf[i] = (u32x4){0, 0, 0, 0};
    }
  }
#pragma unroll
  for (int i = 0; i < 20; i += 4) asm volatile("" : "+v"(wf[i]), "+v"(wf[i + 1]), "+v"(wf[i + 2]), "+v"(wf[i + 3]));
  u32 kmax = 0;
  int win = 0;
  if (w == 0) {
    DRAIN();
    u32x4 ck;
    const u32 co = O_CAND + xc * CAND_REP + l * 16;
#pragma nounroll
    for (int sp = 0; sp < SPIN; ++sp) {
      REREAD();
#pragma unroll
      for (int i = 0; i < 4; ++i) ck[i] = ld4(rs, co + i * 1024);
      if (!__any(umax4(ck) == SENT)) break;
      RETRY(0x100);
    }
    const u32 mine = umax4(ck);
    const int wi_ = l + (ck[0] == mine ? 0 : (ck[1] == mine ? 64 : (ck[2] == mine ? 128 : 192)));
    u32 v = mine;
    v = max(v, dppu<0xB1>(v)); v = max(v, dppu<0x4E>(v)); v = max(v, dppu<0x141>(v)); v = max(v, dppu<0x140>(v));
    kmax = max(max(__builtin_amdgcn_readlane(v, 0), __builtin_amdgcn_readlane(v, 16)), max(__builtin_amdgcn_readlane(v, 32), __builtin_amdgcn_readlane(v, 48)));
    win = __builtin_amdgcn_readlane(wi_, __builtin_ctzll(__ballot(mine == kmax)));
    STAMP(12);
  }
  if (gu) {
    if (w == 0) {
      const u32 ho = O_H2 + win * 1280 + l * 16, ho2 = l < 16 ? ho + 1024 : NONE;
      u32x4 a, e;
#pragma nounroll
      for (int sp = 0; sp < SPIN; ++sp) {
        REREAD();
        a = ld16(rs, ho); e = ld16(rs, ho2);
        *(u32x4*)(L + L_H + l * 4) = a;
        if (l < 16) *(u32x4*)(L + L_H + 256 + l * 4) = e;
        if (!__any(max(umax4(a), umax4(e)) == SENT)) break;
        RETRY(0x10000);
      }
    }
    BAR();
    STAMP(13);
    f32x4 d = z4;
#pragma unroll
    for (int i = 0; i < 20; ++i) d = mfma16(*(const u32x4*)(L + L_H + i * 16 + gam * 4), wf[i], d);
    const float gg = bfr(d[0]), av = bfr(gg * sigm(gg)) * bfr(dppf<0x128>(d[0]));
    const float o = dppf<0xB1>(av);
    st4(rs, m16 < 8 ? O_ACT + (gam + 4 * (l & 1)) * ACT_REP + (xc * 6 + j) * 64 + g * 16 + (m16 >> 1) * 4 : NONE, (l & 1) ? pk(o, av) : pk(av, o));
  } else {
    const int dni = xc * 5 + j - 6;
    const rsrc_t ro = __builtin_amdgcn_make_buffer_rsrc(out, 0, S * D * 2, 0x00020000);
    const u32 oo = l < 8 ? (16 * (win & 15) + (win >> 4) + 256 * (kmax & 7)) * 1280 + dni * 32 + l * 4 : NONE;
    f32x2 base = {0, 0}, gs = {0, 0};
    if (w == 0) {
#pragma nounroll
      for (int sp = 0; sp < SPIN; ++sp) {
        REREAD();
        if (!__any(ld4(rs, O_DONE + win * 16) == SENT)) break;
      }
      REREAD();
      base = up2(ld4(ro, oo));
      const u32 rb = (kmax & 0x80000000u) ? (kmax & 0x7FFFFFFFu) : ~kmax;
      gs = up2(L[L_M + 960 + dni * 8 + (l & 7)]) * bfr(sigm(bfr(asf(rb))));
      keep(base); keep(gs);
    } else {
      __builtin_amdgcn_s_sleep(SLEEP_C);
    }
    const u32 ao = m16 < 12 ? O_ACT + xc * ACT_REP + (12 * g + m16) * 64 + gam * 16 : NONE;
    u32x4 a;
#pragma nounroll
    for (int sp = 0; sp < SPIN; ++sp) {
      REREAD();
      a = ld16(rs, ao);
      if (!__any(umax4(a) == SENT)) break;
      RETRY(0x10000);
    }
    STAMP(13);
    float s = 0;
#pragma unroll
    for (int r = 0; r < 4; ++r) {
      const f32x4 d0 = mfma16(a, wf[r], z4), d1 = mfma16(a, wf[4 + r], z4), d2 = mfma16(a, wf[8 + r], z4);
      s += gam == 0 ? d0[r] : (gam == 1 ? d1[r] : d2[r]);
    }
    s = fold32(s, s);
    s = fold16(s, s);
    if (l < 16) Lf[L_Q + 16 + g * 16 + l] = s;
    BAR();
    if (w == 0) {
      const float* p = Lf + L_Q + 16 + (l & 7) * 2;
      const f32x2 y = (*(const f32x2*)p + *(const f32x2*)(p + 16)) + (*(const f32x2*)(p + 32) + *(const f32x2*)(p + 48));
      const f32x2 o = base + gs * y;
      st4(ro, oo, pk(o[0], o[1]));
    }
  }
  STAMP(14);
  STAMP_OUT();
}

extern "C" void kernel_launch(void* const* d_in, const int* in_sizes, int n_in, void* d_out, int out_size, void* d_ws, size_t ws_size,
                              hipStream_t stream) {
  fused<<<256, 512, 0, stream>>>(Args{(const u16*)d_in[0], (const u16*)d_in[8], (const u16*)d_in[9], (char*)d_ws, (const u16*)d_in[1], (const u16*)d_in[2],
                                 (const u16*)d_in[4], (const u16*)d_in[5], (const u16*)d_in[6], (const u16*)d_in[7], (u16*)d_out});
}
```

```cpp
#include <hip/hip_runtime.h>
#include <stdint.h>

typedef unsigned u32;
typedef unsigned short u16;
typedef unsigned long long u64;
typedef u32 u32x2 __attribute__((ext_vector_type(2)));
typedef u32 u32x4 __attribute__((ext_vector_type(4)));
typedef u32 u32x2u __attribute__((ext_vector_type(2), aligned(2)));
typedef float f32x2 __attribute__((ext_vector_type(2)));
typedef float f32x4 __attribute__((ext_vector_type(4)));
typedef float f32x16 __attribute__((ext_vector_type(16)));
typedef short s16x4 __attribute__((ext_vector_type(4)));
typedef __bf16 bf16x2 __attribute__((ext_vector_type(2)));
typedef __bf16 bf16x8 __attribute__((ext_vector_type(8)));
typedef __amdgpu_buffer_rsrc_t rsrc_t;
#define DI __device__ __forceinline__
#define LAS __attribute__((address_space(3)))
#define GAS __attribute__((address_space(1)))
#define NWAVES 8

constexpr int S = 2048, D = 640, DF = 321, F = 1536;
constexpr float EPS = 1e-5f;
constexpr u32 SENT = 0xFFFFFFFFu, NONE = 0xFFFFFF00u;
constexpr int SPIN = 1 << 16;

constexpr u32 O_EPOCH = 0;
constexpr u32 O_TOK = 212992, TOK_REP = 2048;
constexpr u32 O_DONE = 8192;
constexpr u32 O_CAND = 16384, CAND_REP = 4096;
constexpr u32 O_ACT = 49152, ACT_REP = 4096;
constexpr u32 O_MOD = 81920, MOD_REP = 16384;
constexpr u32 O_H2 = 262144;
constexpr u32 SEGB = 16384 + 2304;
constexpr u32 O_T = 1u << 20;
constexpr u32 O_STAMP = 7u << 20;
constexpr u32 WS_USED = 8u << 20;

constexpr int L_U = 0;
constexpr int L_M = 2560;
constexpr int L_Q = 3840;
constexpr int L_H = L_U;
constexpr int L_G = 4672;
constexpr int L_T = 14912;
constexpr int L_Z = L_T;
constexpr int L_W = L_G;
constexpr int L_X1 = L_G + 4096;
constexpr int L_W2 = L_G + 8192;
constexpr int L_X2 = L_T + 5120;
constexpr int L_D = 35392;
constexpr int L_END = 35456;

DI u32 pk(float lo, float hi) { return __builtin_bit_cast(u32, __builtin_convertvector((f32x2){lo, hi}, bf16x2)); }
DI float asf(u32 v) { return __builtin_bit_cast(float, v); }
DI u32 asu(float v) { return __builtin_bit_cast(u32, v); }
DI float lo16(u32 v) { return asf(v << 16); }
DI float hi16(u32 v) { return asf(v & 0xffff0000u); }
DI f32x2 up2(u32 v) { return (f32x2){lo16(v), hi16(v)}; }
DI float bfr(float x) { return lo16(pk(x, 0.f)); }
DI float ldbf(const u16* p) { return lo16(*p); }
DI f32x16 mfma32(u32x4 a, u32x4 b, f32x16 c) {
  return __builtin_amdgcn_mfma_f32_32x32x16_bf16(__builtin_bit_cast(bf16x8, a), __builtin_bit_cast(bf16x8, b), c, 0, 0, 0);
}
DI f32x4 mfma16(u32x4 a, u32x4 b, f32x4 c) {
  return __builtin_amdgcn_mfma_f32_16x16x32_bf16(__builtin_bit_cast(bf16x8, a), __builtin_bit_cast(bf16x8, b), c, 0, 0, 0);
}
DI float dot2(u32 a, u32 b, float c) {
  return __builtin_amdgcn_fdot2_f32_bf16(__builtin_bit_cast(bf16x2, a), __builtin_bit_cast(bf16x2, b), c, false);
}
DI float cosr(float rev) { return __builtin_amdgcn_cosf(rev); }
DI float sinr(float rev) { return __builtin_amdgcn_sinf(rev); }
template <int CTRL> DI u32 dppu(u32 v) { return (u32)__builtin_amdgcn_update_dpp(0, (int)v, CTRL, 0xf, 0xf, true); }
template <int CTRL> DI float dppf(float v) { return asf(dppu<CTRL>(asu(v))); }
DI float row_sum(float v) {
  v += dppf<0xB1>(v); v += dppf<0x4E>(v); v += dppf<0x141>(v); v += dppf<0x140>(v);
  return v;
}
DI float fold32(float a, float b) { auto r = __builtin_amdgcn_permlane32_swap(asu(a), asu(b), false, false); return asf(r[0]) + asf(r[1]); }
DI float fold16(float a, float b) { auto r = __builtin_amdgcn_permlane16_swap(asu(a), asu(b), false, false); return asf(r[0]) + asf(r[1]); }
DI float rdl(float v, int lane) { return asf(__builtin_amdgcn_readlane(asu(v), lane)); }
DI float sigm(float x) { return __builtin_amdgcn_rcpf(1.f + __expf(-x)); }
DI u32 umax4(u32x4 v) { return max(max(v[0], v[1]), max(v[2], v[3])); }
template <class V> DI f32x2 pr(const V& v, int i) { return (f32x2){v[i], v[i + 1]}; }
template <bool SWAP = false> DI void cmul2(f32x2 r, f32x2 im, f32x2 c, f32x2 s, u32& o0, u32& o1) {
  const f32x2 a = r * c - im * s, b = im * c + r * s;
  o0 = SWAP ? pk(b[0], a[0]) : pk(a[0], b[0]); o1 = SWAP ? pk(b[1], a[1]) : pk(a[1], b[1]);
}
struct Ph { f32x2 c, s; };
DI Ph phasor(float a, float b) { return {(f32x2){cosr(a), cosr(b)}, (f32x2){sinr(a), sinr(b)}}; }
DI Ph phasor(float a) { const float c = cosr(a), s = sinr(a); return {(f32x2){c, c}, (f32x2){s, s}}; }
DI Ph rot(Ph p, Ph r) { return {p.c * r.c - p.s * r.s, p.s * r.c + p.c * r.s}; }
DI u32x4 trfrag(const LAS u32* p, const LAS u32* q) {
  const u32x2 a = __builtin_bit_cast(u32x2, __builtin_amdgcn_ds_read_tr16_b64_v4i16((LAS s16x4*)p));
  const u32x2 b = __builtin_bit_cast(u32x2, __builtin_amdgcn_ds_read_tr16_b64_v4i16((LAS s16x4*)q));
  return (u32x4){a[0], a[1], b[0], b[1]};
}

DI u32x4 ld16(rsrc_t r, u32 o) { return __builtin_amdgcn_raw_buffer_load_b128(r, o, 0, 16); }
DI u32x2 ld8(rsrc_t r, u32 o) { return __builtin_amdgcn_raw_buffer_load_b64(r, o, 0, 16); }
DI u32 ld4(rsrc_t r, u32 o) { return __builtin_amdgcn_raw_buffer_load_b32(r, o, 0, 16); }
DI void st16(rsrc_t r, u32 o, u32x4 v) { __builtin_amdgcn_raw_buffer_store_b128(v, r, o, 0, 16); }
DI void st8(rsrc_t r, u32 o, u32x2 v) { __builtin_amdgcn_raw_buffer_store_b64(v, r, o, 0, 16); }
DI void st4(rsrc_t r, u32 o, u32 v) { __builtin_amdgcn_raw_buffer_store_b32(v, r, o, 0, 16); }
#define DRAIN() asm volatile("s_waitcnt vmcnt(0)" ::: "memory")
#define BAR() do { __builtin_amdgcn_sched_barrier(0); asm volatile("s_waitcnt lgkmcnt(0)\n\ts_barrier" ::: "memory"); __builtin_amdgcn_sched_barrier(0); } while (0)
#define REREAD() asm volatile("" ::: "memory")
#define PIN() __builtin_amdgcn_sched_barrier(0)
#define PRIO(n) __builtin_amdgcn_s_setprio(n)
#define POLL(ok) { u32 n_ = SPIN, lim_, m_; _Pragma("nounroll") do { lim_ = --n_ ? (ok) : SENT; asm volatile("" : "+v"(lim_)::"memory");
#define UNTIL(m, r) m_ = (m); RETRY(r); } while (__any(m_ > lim_)); RETRY(-(r)); }
#define UNLIKELY(x) __builtin_expect(!!(x), 0)
template <class T> DI void keep(T& v) { asm volatile("" : "+v"(v)); }
DI void glds16(const void* base, u32 off, u32 lds_byte) {
  u32 m;
  asm volatile("s_mov_b32 %0, m0\n\ts_mov_b32 m0, %3\n\ts_nop 0\n\tglobal_load_lds_dwordx4 %1, %2\n\ts_mov_b32 m0, %0"
               : "=&s"(m) : "v"(off), "s"(base), "s"(lds_byte) : "memory");
}
DI void warm(u64 base, u32 bytes, u32 off, u32 lds_byte) {
  const u32x4 d = {(u32)base, (u32)(base >> 32) & 0xffffu, bytes, 0x00020000u};
  u32 m;
  asm volatile("s_mov_b32 %0, m0\n\ts_mov_b32 m0, %3\n\ts_nop 0\n\tbuffer_load_dword %1, %2, 0 offen lds\n\ts_mov_b32 m0, %0"
               : "=&s"(m) : "v"(off), "s"(d), "s"(lds_byte) : "memory");
}
#define STAMP(i)
#define STAMP_OUT()
#define RETRY(v)

#define WFIRST 2
#define CLR_POS 0
#define PF_MOD 1
#define OUT_MODE 1
#define SLEEP_O 16
#define SLEEP_GU 3
#define SLEEP_DN 3
#define GU_FROM 0
#define B3_EARLY 0
#define SLEEP_C0 0
#define X_NOW 0
#define X_NOMLP 0
#define X_NOMOD 0
#define B3_SLEEP 0
#define SLEEP_M2 32
#define PF_GU 3
#define PF_DN 0
#define SLEEP_X 6
#define SLEEP_W 10
#define B6_SLEEP 0
#define SLEEP_M 0
#define SLEEP_A 0
#define SLEEP_C 28

__global__ __launch_bounds__(512, 2) void fused(const u16* __restrict__ x, const u16* __restrict__ cvec, const u16* __restrict__ wmod, char* ws,
                                                const u16* __restrict__ fwr, const u16* __restrict__ fwi, const u16* __restrict__ wrt,
                                                const u16* __restrict__ wg, const u16* __restrict__ wu, const u16* __restrict__ wd, u16* out) {
  __shared__ __attribute__((aligned(16))) u32 L[L_END];
  STAMP(0);
  u64 code_ptr;
  u32 code_len;
  asm volatile("s_getpc_b64 %0\n.Lcode_from:\n\ts_mov_b32 %1, .Lcode_to-.Lcode_from-4\n\t.subsection 1\n.Lcode_to:\n\t.subsection 0"
               : "=s"(code_ptr), "=s"(code_len));
  const u32 lds0 = (u32)(size_t)(LAS u32*)L, ldsd = lds0 + L_D * 4;
  warm(code_ptr, code_len, (threadIdx.x * 4 + ((blockIdx.x >> 3) & 3)) * 128u, ldsd);
  asm volatile("" ::"s"(x), "s"(cvec), "s"(wmod), "s"(ws), "s"(fwr), "s"(fwi), "s"(wrt), "s"(wg), "s"(wu), "s"(wd), "s"(out));
  const int b = blockIdx.x, xc = b & 7, j = b >> 3, t = threadIdx.x, l = t & 63;
  const int w = __builtin_amdgcn_readfirstlane(t >> 6), w4 = w & 3;
  const int pos = xc * 32 + j, s2 = 16 * (pos & 15) + (pos >> 4);
  const bool big = (xc & 3) == 0;
  const int cidx = j - (big ? 8 : 12), x6 = xc - 1 - (xc > 4), x2 = xc >> 2;
  const bool col = cidx >= 0, hasmod = j < (big ? 4 : 12), gu = !big && j < 8;
  const int mb0 = big ? 72 + 4 * x2 : 12 * x6, mb = mb0 + j, gui = 8 * x6 + j, dn0 = big ? 24 + 8 * x2 : 4 * x6, dni = dn0 + (big ? j : j - 8);
  const int m16 = l & 15, gam = l >> 4;
  const rsrc_t rs = __builtin_amdgcn_make_buffer_rsrc(ws, 0, WS_USED, 0x00020000);
  float* Lf = (float*)L;

  u32x2 xa[2], xb[2], wra, wrb;
  u32 xd[2], wrd;
  u32x4 cf4;
  u64 tag = *(const __attribute__((address_space(4))) u64*)(size_t)(ws + O_EPOCH);
  if (w >= 4) {
    if (SLEEP_X && col) __builtin_amdgcn_s_sleep(SLEEP_X);
#pragma unroll
    for (int i = 0; i < 2; ++i) {
      const u16* xrow = x + (size_t)(s2 + 256 * w4 + 1024 * i) * D;
      xa[i] = *(const u32x2*)(xrow + 4 * l); xb[i] = *(const u32x2*)(xrow + 256 + 4 * l); xd[i] = *(const u32*)(xrow + 512 + 2 * l);
    }
    PIN();
    if (hasmod) {
      asm volatile("global_load_dwordx4 %0, %1, %2" : "=v"(cf4) : "v"(320 * w4 + 64 * min(m16, 4) + 16 * gam), "s"(cvec) : "memory");
      const u32 o = (160 * w4 + ((l >> 1) ^ ((l >> 2) & 4))) * 7680 + mb * 32 + (l & 1) * 16;
      int k = mb & 3;
#pragma unroll
      for (int i = 0; i < 5; ++i) { glds16(wmod, o + k * (32 * 7680), lds0 + (L_G + w4 * 2560 + k * 256) * 4); k = k == 4 ? 0 : k + 1; }
    }
  }
  if (w < 2 && col) warm((u64)wmod, 640 * 7680, (t & 3) < (big ? 1 : 3) ? (32 * cidx + (t >> 2)) * 7680 + 32 * mb0 + (t & 3) * 128 : NONE, ldsd);
  PIN();
  const int k1 = xc <= 4 ? xc : 8 - xc, th0 = big ? (cidx * 43) >> 7 : (cidx * 26) >> 7, kg = cidx - th0 * (big ? 3 : 5), th = th0 + (xc > 4 ? 4 : 0);
  const int kap0 = 4 * kg, lam0 = 4 * th, seg = (big ? th * 6 + 3 * x2 : 8 + 40 * k1 + th * 5) + kg;
  const bool mir = lam0 >= 16, herm = col && kap0 == 0 && (lam0 & 15) == 0;
  const f32x4 z4 = {0, 0, 0, 0};
  const int t4 = t & 255;
  auto warm_mod = [&]() { warm((u64)wmod, 640 * 7680, (t4 & 7) < (big ? 2 : 6) ? (32 * cidx + (t4 >> 3)) * 7680 + 2560 + 64 * mb0 + 128 * (t4 & 7) : NONE, ldsd); };
  auto warm_gu = [&]() {
    const int row = 32 * cidx + ((t4 & 127) >> 2);
    if (!big) warm((u64)(w4 < 2 ? wg : wu), D * F * 2, row % 80 >= GU_FROM ? row * (F * 2) + 512 * x6 + 128 * (t4 & 3) : NONE, ldsd);
  };
  auto warm_dn = [&]() { warm((u64)wd, D * F * 2, t4 < 154 && (big || !(t4 & 1)) ? (77 * cidx + (t4 >> 1)) * (D * 2) + 32 * dn0 + 128 * (t4 & 1) : NONE, ldsd); };

  if (w >= 4) {
    const int tr = t - 256;
    auto gemv5 = [&](const LAS u32* p, const LAS u32* q, int step, float* o) {
      u32x4 f[5];
      f32x4 d[5];
#pragma unroll
      for (int i = 0; i < 5; ++i) f[i] = trfrag(p + i * step, q + i * step);
      PIN();
#pragma unroll
      for (int i = 0; i < 5; ++i) d[i] = mfma16(cf4, f[i], z4);
      const float lo = (d[0][0] + d[1][1]) + (d[2][2] + d[3][3]);
      o[w4 * 64 + l] = gam ? d[4][0] : lo;
    };
    auto mlp_dma = [&]() {
      if (gu) {
        const u32 o = (160 * w4 + (l >> 2)) * (F * 2) + gui * 64 + ((l ^ (l >> 4)) & 2) * 16 + (l & 1) * 16;
        int k = 5 * (j & 1);
#pragma nounroll
        for (int i = 0; i < 10; ++i) {
          glds16(wg, o + k * (16 * F * 2), lds0 + (L_T + w4 * 2560 + k * 256) * 4);
          glds16(wu, (o ^ 16) + k * (16 * F * 2), lds0 + (L_T + 10240 + w4 * 2560 + k * 256) * 4);
          k = k == 9 ? 0 : k + 1;
        }
      } else {
        const u32 o = (384 * w4 + ((l >> 1) ^ ((l >> 2) & 4))) * (D * 2) + dni * 32 + (l & 1) * 16;
        int k = 3 * (j & 3);
#pragma nounroll
        for (int i = 0; i < 12; ++i) { glds16(wd, o + k * (32 * D * 2), lds0 + (L_T + w4 * 3072 + k * 256) * 4); k = k == 11 ? 0 : k + 1; }
      }
    };
    if (hasmod) {
      PRIO(2);
      asm volatile("s_waitcnt vmcnt(5)" : "+v"(cf4)::"memory");
#pragma unroll
      for (int q = 0; q < 4; ++q) { const float a = lo16(cf4[q]), e = hi16(cf4[q]); cf4[q] = pk(a * sigm(a), e * sigm(e)); }
      keep(cf4);
      const LAS u32* g32 = (const LAS u32*)(L + L_G) + w4 * 2560 + gam * 64 + m16 * 2;
      keep(g32);
      STAMP(7);
      DRAIN();
      STAMP(8);
      gemv5(g32 + (gam & 1) * 32, g32 + (~gam & 1) * 32, 256, Lf + L_Q + 16);
      BAR();
    }
    wra = *(const u32x2*)(wrt + 4 * l); wrb = *(const u32x2*)(wrt + 256 + 4 * l); wrd = *(const u32*)(wrt + 512 + 2 * l);
    u32x4 B1r[4], B1i[4], A4r[4], A4i[4], Fr, Fi;
    const float pf = (float)m16 * (1.f / 256.f);
    if (col) {
      const float a0 = (float)(16 * gam + 4 * w4) * pf;
      Ph p[2] = {phasor(a0, a0 + pf), phasor(a0 + 2.f * pf, a0 + 3.f * pf)};
      const Ph st = phasor(64.f * pf);
#pragma unroll
      for (int q = 0; q < 4; ++q)
#pragma unroll
        for (int e = 0; e < 2; ++e) {
          if (q) p[e] = rot(p[e], st);
          const f32x2 n = -p[e].s;
#pragma unroll
          for (int k = 0; k < 2; ++k) { B1r[2 * e + k][q] = pk(p[e].c[k], p[e].s[k]); B1i[2 * e + k][q] = pk(n[k], p[e].c[k]); }
        }
#pragma unroll
      for (int q = 0; q < 4; ++q) { keep(B1r[q]); keep(B1i[q]); }
    }
    PIN();
    f32x2 xv[2][5];
    {
      float s[2], q[2];
#pragma unroll
      for (int i = 0; i < 2; ++i) {
        xv[i][0] = up2(xa[i][0]); xv[i][1] = up2(xa[i][1]); xv[i][2] = up2(xb[i][0]); xv[i][3] = up2(xb[i][1]); xv[i][4] = up2(xd[i]);
        f32x2 a = xv[i][0], c = xv[i][0] * xv[i][0];
#pragma unroll
        for (int k = 1; k < 5; ++k) { a += xv[i][k]; c += xv[i][k] * xv[i][k]; }
        s[i] = a[0] + a[1]; q[i] = c[0] + c[1];
      }
      const float v = row_sum(fold16(fold32(s[0], s[1]), fold32(q[0], q[1]))) * (1.f / D);
      const float m0 = rdl(v, 0), m1 = rdl(v, 32);
      const float r0 = __builtin_amdgcn_rsqf(fmaxf(rdl(v, 16) - m0 * m0, 0.f) + EPS), r1 = __builtin_amdgcn_rsqf(fmaxf(rdl(v, 48) - m1 * m1, 0.f) + EPS);
      f32x2 sm[5], df[5];
#pragma unroll
      for (int k = 0; k < 5; ++k) { const f32x2 z0 = (xv[0][k] - m0) * r0, z1 = (xv[1][k] - m1) * r1; sm[k] = z0 + z1; df[k] = z0 - z1; }
      float* z = Lf + L_Z + w4 * 640;
      *(f32x4*)(z + 4 * l) = (f32x4){sm[0][0], sm[0][1], sm[1][0], sm[1][1]};
      *(f32x4*)(z + 256 + 4 * l) = (f32x4){sm[2][0], sm[2][1], sm[3][0], sm[3][1]};
      *(f32x2*)(z + 512 + 2 * l) = sm[4];
      *(f32x4*)(z + 2560 + 4 * l) = (f32x4){df[0][0], df[0][1], df[1][0], df[1][1]};
      *(f32x4*)(z + 2560 + 256 + 4 * l) = (f32x4){df[2][0], df[2][1], df[3][0], df[3][1]};
      *(f32x2*)(z + 2560 + 512 + 2 * l) = df[4];
    }
    if (tr == 0) *(u32x2*)(L + L_Q) = (u32x2){0u, 0u};
    STAMP(1);
    asm volatile("" : "+s"(tag)::"memory");
    PRIO(2);
    BAR();
    const u32 tlo = (u32)(tag + 1), thi = (u32)((tag + 1) >> 32);
    {
      const int h = l >> 5, lp = l & 31;
      const float* z = Lf + L_Z + h * 2560 + 160 * w4;
      const float e = h ? 0.f : 1.f, kk = h ? 0.70710678f : 0.f, ek = e + kk, sg = h ? -1.f : 1.f, ne = 1.f - e;
      f32x4 z4[4]; float z1[4];
#pragma unroll
      for (int i = 0; i < 4; ++i) { z4[i] = *(const f32x4*)(z + i * 640 + 4 * lp); z1[i] = z[i * 640 + 128 + lp]; }
      f32x2 p0[5], p1[5];
#pragma unroll
      for (int k = 0; k < 5; ++k) {
        const float a = k < 4 ? z4[0][k] : z1[0], c = k < 4 ? z4[1][k] : z1[1], bb = k < 4 ? z4[2][k] : z1[2], d = k < 4 ? z4[3][k] : z1[3];
        const float sm = c + d, df = c - d;
        p0[k] = (f32x2){a + e * (bb + sm) + kk * df, e * a + sg * bb - ek * sm};
        p1[k] = (f32x2){a - e * bb - kk * df, ne * bb - kk * sm - e * df};
        keep(p0[k]); keep(p1[k]);
      }
      if (SLEEP_M) __builtin_amdgcn_s_sleep(SLEEP_M);
      const u32 mo = O_MOD + xc * MOD_REP + 640 * w4;
      const u32 o4 = mo + 16 * lp, o1 = mo + 512 + 8 * (lp >> 1), want = (lp & 2) ? thi : tlo;
      const u32 hm = h ? 0u : ~0u;
      u32* u = L + L_U + h * 640 + 5 * w4;
      u32* ua = u + (lp & 7) * 20 + (lp >> 3);
      u32* ub = u + ((lp >> 2) + 8 * (lp & 3)) * 20 + 4;
      u32x4 c4, s4; u32x2 c1, s1;
      STAMP(2);
      POLL(0u)
        c4 = ld16(rs, o4 + 2560); c1 = ld8(rs, o1 + 2560);
        s4 = ld16(rs, h ? NONE : o4); s1 = ld8(rs, h ? NONE : o1);
      UNTIL((c4[1] ^ tlo) | (c4[3] ^ thi) | (c1[1] ^ want) | (((s4[1] ^ tlo) | (s4[3] ^ thi) | (s1[1] ^ want)) & hm), 0x100)
      STAMP(3);
      const float A[5] = {lo16(c4[0]), hi16(c4[0]), lo16(c4[2]), hi16(c4[2]), (lp & 1) ? hi16(c1[0]) : lo16(c1[0])};
      const float B[5] = {lo16(s4[0]), hi16(s4[0]), lo16(s4[2]), hi16(s4[2]), (lp & 1) ? hi16(s1[0]) : lo16(s1[0])};
#pragma unroll
      for (int k = 0; k < 5; ++k) {
        const f32x2 q1 = p1[k] * A[k];
        u32* o = k < 4 ? ua + k * 160 : ub;
        o[0] = pk(p0[k][0] * A[k] + B[k], p0[k][1] * A[k]);
        o[1280] = pk(q1[0], q1[1]);
      }
    }
    BAR();
    PRIO(0);
    STAMP(4);
    if (hasmod) {
      const u32 o = (160 * w4 + (l >> 2)) * 7680 + 2560 + mb * 64 + ((l ^ (l >> 4)) & 2) * 16 + (l & 1) * 16;
      int k = 5 * (mb & 1);
#pragma nounroll
      for (int i = 0; i < (X_NOMOD ? 0 : 10); ++i) { glds16(wmod, o + k * (16 * 7680), lds0 + (L_G + w4 * 2560 + k * 256) * 4); k = k == 9 ? 0 : k + 1; }
      DRAIN();
      const LAS u32* p = (const LAS u32*)(L + L_G) + w4 * 2560 + gam * 128 + (m16 >> 2) * 16 + (m16 & 3) * 2;
      gemv5(p + (gam & 1) * 8, p + (gam & 1) * 8 + 64, 512, Lf + L_Q + 16);
      gemv5(p + (~gam & 1) * 8, p + (~gam & 1) * 8 + 64, 512, Lf + L_Q + 272);
    }
    if (col) {
      const u32 to = w4 == 3 ? O_TOK + xc * TOK_REP + l * 16 : NONE - 1024;
      u32x4 tg = ld16(rs, to), te = ld16(rs, to + 1024);
      PIN();
      const float af = (float)(16 * ((m16 >> 2) + 4 * (m16 & 3)) + 4 * w4) * (1.f / 256.f);
      const float g4 = (float)(4 * gam);
#pragma unroll
      for (int e = 0; e < 2; ++e) {
        const float a0 = af + (float)(2 * e) * (1.f / 256.f), a1 = af + (float)(2 * e + 1) * (1.f / 256.f);
        Ph p = phasor(g4 * a0, g4 * a1);
        const Ph st = phasor(a0, a1);
#pragma unroll
        for (int q = 0; q < 4; ++q) {
          if (q) p = rot(p, st);
          const f32x2 n = -p.s;
#pragma unroll
          for (int k = 0; k < 2; ++k) { A4r[2 * e + k][q] = pk(p.c[k], n[k]); A4i[2 * e + k][q] = pk(p.s[k], p.c[k]); }
        }
        const Ph f = phasor((g4 + (float)(2 * e)) * (16.f * pf), (g4 + (float)(2 * e + 1)) * (16.f * pf));
        const f32x2 n = -f.s;
#pragma unroll
        for (int k = 0; k < 2; ++k) { Fr[2 * e + k] = pk(f.c[k], f.s[k]); Fi[2 * e + k] = pk(n[k], f.c[k]); }
      }
#pragma unroll
      for (int q = 0; q < 4; ++q) { keep(A4r[q]); keep(A4i[q]); }
      keep(Fr); keep(Fi);
      auto miss = [&]() { return (tg[0] ^ tlo) | (tg[1] ^ thi) | (tg[2] ^ tlo) | (tg[3] ^ thi) | (te[0] ^ tlo) | (te[1] ^ thi) | (te[2] ^ tlo) | (te[3] ^ thi); };
      if (w4 == 3 && UNLIKELY(__any(miss() != 0))) {
        POLL(0u)
          __builtin_amdgcn_s_sleep(4);
          tg = ld16(rs, to); te = ld16(rs, to + 1024);
        UNTIL(miss(), 0x10000)
      }
    }
    STAMP(5);
    BAR();
    STAMP(6);
    if (col) {
      PRIO(2);
      const u32 o = O_T + seg * SEGB + w4 * 4096 + l * 4;
      u32x4 in[4];
#pragma unroll
      for (int i = 0; i < 16; ++i) in[i >> 2][i & 3] = ld4(rs, o + i * 256);
      BAR();
      f32x4 fr[4], fi[4];
      {
        const bool hw = herm && w4 == 0;
        const float h0 = ((k1 & 3) == 0 && kap0 == 0 ? 0.5f : 1.f) * (hw ? 0.5f : 1.f), h2 = (k1 & 3) == 0 && kap0 == 8 ? 0.5f : 1.f;
        u32x4 v[4]; u32 e[4];
#pragma unroll
        for (int r = 0; r < 4; ++r) {
          v[r] = *(const u32x4*)(L + L_W + (m16 + 16 * (4 * gam + r)) * 16 + w4 * 4);
          e[r] = L[L_W2 + m16 + 16 * (4 * gam + r)] & (hw ? ~0u : 0u);
        }
#pragma unroll
        for (int r = 0; r < 4; ++r) {
#pragma unroll
          for (int c = 0; c < 4; ++c) { fr[c][r] = lo16(v[r][c]); fi[c][r] = hi16(v[r][c]); }
          fr[0][r] = (fr[0][r] + lo16(e[r])) * h0; fi[0][r] = (fi[0][r] + hi16(e[r])) * h0;
          fr[2][r] *= h2; fi[2][r] *= h2;
        }
      }
#pragma unroll
      for (int c = 0; c < 4; ++c) { keep(fr[c]); keep(fi[c]); }
      PIN();
      const f32x4 z = {0, 0, 0, 0};
      f32x4 yr[4], yi[4];
      u32 top = 0;
#pragma unroll
      for (int bl = 0; bl < 4; ++bl) {
        yr[bl] = mfma16(in[bl], B1r[bl], z); yi[bl] = mfma16(in[bl], B1i[bl], z);
        top = max(max(top, in[bl][0]), in[bl][1]); top = max(max(top, in[bl][2]), in[bl][3]);
        PIN();
      }
      if (UNLIKELY(__any(top == SENT))) {
        RETRY(1);
        POLL(SENT - 1)
#pragma unroll
          for (int i = 0; i < 16; ++i) in[i >> 2][i & 3] = ld4(rs, o + i * 256);
        UNTIL(max(max(umax4(in[0]), umax4(in[1])), max(umax4(in[2]), umax4(in[3]))), 1)
#pragma unroll
        for (int bl = 0; bl < 4; ++bl) { yr[bl] = mfma16(in[bl], B1r[bl], z); yi[bl] = mfma16(in[bl], B1i[bl], z); }
      }
      STAMP(7);
      u32* x1w = L + L_X1 + gam * 1024 + w4 * 64 + m16 * 4;
#pragma unroll
      for (int q = 0; q < 4; ++q)
        *(u32x4*)(x1w + q * 256) = (u32x4){pk(yr[0][q], yi[0][q]), pk(yr[1][q], yi[1][q]), pk(yr[2][q], yi[2][q]), pk(yr[3][q], yi[3][q])};
      BAR();
      u32x4 a[4];
#pragma unroll
      for (int c = 0; c < 4; ++c) a[c] = *(const u32x4*)(L + L_X1 + c * 1024 + w4 * 256 + l * 4);
#pragma unroll
      for (int c = 0; c < 4; ++c) { yr[c] = mfma16(Fr, a[c], z); yi[c] = mfma16(Fi, a[c], z); }
#pragma unroll
      for (int c = 0; c < 4; ++c) {
        u32 o0, o1, o2, o3;
        cmul2<true>(pr(yr[c], 0), pr(yi[c], 0), pr(fr[c], 0), pr(fi[c], 0), o0, o1);
        cmul2<true>(pr(yr[c], 2), pr(yi[c], 2), pr(fr[c], 2), pr(fi[c], 2), o2, o3);
        a[c] = (u32x4){o0, o1, o2, o3};
        yr[c] = mfma16(a[c], Fi, z); yi[c] = mfma16(a[c], Fr, z);
      }
      u32* x2w = L + L_X2 + m16 * 256 + gam * 64;
#pragma unroll
      for (int c = 0; c < 4; ++c)
        *(u32x4*)(x2w + (((4 * c + w4) ^ m16) << 2)) =
            (u32x4){pk(yr[c][0], yi[c][0]), pk(yr[c][1], yi[c][1]), pk(yr[c][2], yi[c][2]), pk(yr[c][3], yi[c][3])};
      BAR();
#pragma unroll
      for (int bl = 0; bl < 4; ++bl) a[bl] = *(const u32x4*)(L + L_X2 + (4 * w4 + bl) * 256 + gam * 64 + ((m16 ^ (4 * w4 + bl)) << 2));
#pragma unroll
      for (int bl = 0; bl < 4; ++bl) { yr[bl] = mfma16(A4r[bl], a[bl], z); yi[bl] = mfma16(A4i[bl], a[bl], z); }
#pragma unroll
      for (int i = 0; i < 16; ++i) st4(rs, o + i * 256, ~(pk(yi[i >> 2][i & 3], yr[i >> 2][i & 3]) ^ in[i >> 2][i & 3]));
      PRIO(0);
      STAMP(8);
      DRAIN();
      if (B6_SLEEP) __builtin_amdgcn_s_sleep(B6_SLEEP);
      STAMP(9);
    } else {
      if (gu) __builtin_amdgcn_s_sleep(SLEEP_GU); else __builtin_amdgcn_s_sleep(SLEEP_DN);
      if (!X_NOMLP) mlp_dma();
      STAMP(9);
    }
    BAR();
    f32x2 ga[5], cb[5], sb[5], gb[5], vv[5];
    float sv, sbw;
    {
      auto ld5 = [&](const u32* p, f32x2 (&v)[5]) {
        const u32x2 a = *(const u32x2*)(p + 2 * l), c = *(const u32x2*)(p + 128 + 2 * l);
        v[0] = up2(a[0]); v[1] = up2(a[1]); v[2] = up2(c[0]); v[3] = up2(c[1]); v[4] = up2(p[256 + l]);
      };
      ld5(L + L_M, ga); ld5(L + L_M + 320, sb); ld5(L + L_M + 640, cb); ld5(L + L_M + 960, gb);
      vv[0] = up2(wra[0]); vv[1] = up2(wra[1]); vv[2] = up2(wrb[0]); vv[3] = up2(wrb[1]); vv[4] = up2(wrd);
      f32x2 a = {0, 0}, c = {0, 0};
#pragma unroll
      for (int k = 0; k < 5; ++k) { c += sb[k] * vv[k]; vv[k] *= cb[k]; a += vv[k]; ga[k] *= 1.f / ((float)S * (float)D); }
      const float v = fold32(a[0] + a[1], c[0] + c[1]), v2 = row_sum(fold16(v, v));
      sv = rdl(v2, 0); sbw = rdl(v2, 32);
    }
    u32 cf[4];
    cf[0] = pk(2.f, (w4 & 1) ? -2.f : 2.f);
#pragma unroll
    for (int g = 1; g < 4; ++g) { const float th = (float)(w4 * g) * 0.125f; cf[g] = pk(2.f * cosr(th), -2.f * sinr(th)); }
#pragma unroll
    for (int k = 0; k < 5; ++k) { keep(ga[k]); keep(cb[k]); keep(sb[k]); keep(gb[k]); keep(vv[k]); keep(xv[0][k]); keep(xv[1][k]); }
#pragma unroll
    for (int g = 0; g < 4; ++g) keep(cf[g]);
    u32 krow = s2 + 256 * w4 + 1024 * (l >> 5);
    keep(krow); keep(sv); keep(sbw);
    if (!col) DRAIN();
    STAMP(10);
    PRIO(2);
    BAR();
    STAMP(11);
    u32x4 ea[4], eb[4]; u32x2 ec[4];
#pragma unroll
    for (int g = 0; g < 4; ++g) {
      const u32* e = L + L_U + g * 640;
      ea[g] = *(const u32x4*)(e + 4 * l); eb[g] = *(const u32x4*)(e + 256 + 4 * l); ec[g] = *(const u32x2*)(e + 512 + 2 * l);
    }
    PIN();
    f32x2 sa_[2] = {}, qa[2] = {}, da[2] = {};
#pragma unroll
    for (int k = 0; k < 5; ++k) {
      f32x2 ev, od;
#pragma unroll
      for (int i = 0; i < 2; ++i) {
        auto el = [&](int g) { return k < 2 ? ea[g][2 * k + i] : (k < 4 ? eb[g][2 * k - 4 + i] : ec[g][i]); };
        ev[i] = dot2(el(2), cf[2], dot2(el(0), cf[0], 0.f));
        od[i] = dot2(el(3), cf[3], dot2(el(1), cf[1], 0.f));
      }
      xv[0][k] += ga[k] * (ev + od); xv[1][k] += ga[k] * (ev - od);
#pragma unroll
      for (int i = 0; i < 2; ++i) { sa_[i] += xv[i][k]; qa[i] += xv[i][k] * xv[i][k]; da[i] += xv[i][k] * vv[k]; }
    }
    const float dsum = fold32(da[0][0] + da[0][1], da[1][0] + da[1][1]);
    const float sq = row_sum(fold16(fold32(sa_[0][0] + sa_[0][1], sa_[1][0] + sa_[1][1]), fold32(qa[0][0] + qa[0][1], qa[1][0] + qa[1][1]))) * (1.f / D);
    const float dt = row_sum(fold16(dsum, dsum));
    const auto sw = __builtin_amdgcn_permlane16_swap(asu(sq), asu(sq), false, false);
    const float mean = asf(sw[0]), rstd = __builtin_amdgcn_rsqf(fmaxf(asf(sw[1]) - mean * mean, 0.f) + EPS);
    const u32 rb = asu(rstd * (dt - mean * sv) + sbw);
    const u32 key = (min(rb ^ ((u32)((int)rb >> 31) | 0x80000000u), 0xFFFFF000u) & ~0x7FFu) | krow;
    if ((l & 31) == 0) __hip_atomic_fetch_max(L + L_Q, key, __ATOMIC_RELAXED, __HIP_MEMORY_SCOPE_WORKGROUP);
    BAR();
    PRIO(0);
    STAMP(12);
    const int best = (__builtin_amdgcn_readfirstlane(L[L_Q]) >> 8) & 7;
    const rsrc_t ro = __builtin_amdgcn_make_buffer_rsrc(out, 0, S * D * 2, 0x00020000);
    u32 op[2][5];
    auto st_row = [&](rsrc_t r, u32 o, const u32 (&v)[5]) {
      st8(r, o + 8 * l, (u32x2){v[0], v[1]}); st8(r, o + 512 + 8 * l, (u32x2){v[2], v[3]}); st4(r, o + 1024 + 4 * l, v[4]);
    };
#pragma unroll
    for (int i = 0; i < 2; ++i) {
      const float m = rdl(mean, 32 * i), rr = rdl(rstd, 32 * i);
      u32 hp[5];
#pragma unroll
      for (int k = 0; k < 5; ++k) {
        const f32x2 h2 = (xv[i][k] - m) * rr * cb[k] + sb[k], o = xv[i][k] + gb[k] * h2;
        hp[k] = pk(h2[0], h2[1]); op[i][k] = pk(o[0], o[1]);
      }
      if (best == w4 + 4 * i) {
        st_row(rs, O_H2 + s2 * 1280, hp);
        st_row(ro, (s2 + 256 * w4 + 1024 * i) * 1280, op[i]);
      }
    }
    if ((best & 3) == w4) {
      DRAIN();
      if (l == 0) st4(rs, O_DONE + s2 * 16, 1u);
    }
    if (OUT_MODE == 1) {
      if (col) __builtin_amdgcn_s_sleep(SLEEP_O); else BAR();
    }
#pragma unroll
    for (int i = 0; i < 2; ++i)
      if (best != w4 + 4 * i) {
        if (OUT_MODE == 2) {
          u16* orow = out + (size_t)(s2 + 256 * w4 + 1024 * i) * D;
          *(u32x2*)(orow + 4 * l) = (u32x2){op[i][0], op[i][1]}; *(u32x2*)(orow + 256 + 4 * l) = (u32x2){op[i][2], op[i][3]};
          *(u32*)(orow + 512 + 2 * l) = op[i][4];
        } else st_row(ro, (s2 + 256 * w4 + 1024 * i) * 1280, op[i]);
      }
    if (b == 255 && tr == 0) st8(rs, O_EPOCH, (u32x2){tlo, thi});
    STAMP(13);
    STAMP_OUT();
    return;
  }

  const int g = w, c = l & 31, h = l >> 5;
  const int nsg = g == 0 ? 6 : 5;
  const bool act = c < 4 * nsg;
  const u32 slot = act ? O_T + ((g == 0 ? 0 : 8 + 40 * g) + h * nsg + (c >> 2)) * SEGB + pos * 64 + (c & 3) * 16 : NONE;
  const u32 sstep = act ? 2 * nsg * SEGB : 0;
  u32x4 old[4], ar[4], ai[4], cr[3], ci[3];
  f32x2 uc[6], us[6];
  u32 tlo, thi;
  u32 pub_off = O_MOD + (gam + 4 * (l & 1)) * MOD_REP + (m16 >> 1) * 8, pub_sel = (l & 1) ? 0x01000504u : 0x05040100u, pub_tag;
  const LAS float* pub_src = (const LAS float*)(Lf + L_Q + 16) + (l >> 5) * 64 + (l & 31);
  auto publish_mod = [&](int part, int col0) {
    const float mul = col0 < 640 ? 8.f : 1.f, add = (col0 >= 640 && col0 < 1280) || (col0 >= 2560 && col0 < 3200) ? 1.f : 0.f;
    const float s0 = pub_src[part] + pub_src[part + 128], s1 = fold32(s0, s0);
    const u32 v = pk(bfr(fold16(s1, s1)) * mul + add, 0.f);
    __builtin_amdgcn_raw_buffer_store_b64((u32x2){__builtin_amdgcn_perm(dppu<0xB1>(v), v, pub_sel), pub_tag}, rs, pub_off, col0 * 4, 16);
  };
  {
    u32x2 wa[4], wc[4];
    const int kd = kap0 + 20 * (lam0 + (t & 3)), cw = mir ? D - 3 - kd : kd;
    auto ldw = [&](int i) {
      if (X_NOW) { wa[i] = *(const u32x2u*)((const char*)fwr + l * 8); wc[i] = *(const u32x2u*)((const char*)fwi + l * 8 + i * 512); return; }
      const int ks = k1 + 8 * ((t >> 2) + 64 * i);
      const u32 o = ((mir ? (S - ks) & (S - 1) : ks) * DF + cw) * 2;
      wa[i] = *(const u32x2u*)((const char*)fwr + o); wc[i] = *(const u32x2u*)((const char*)fwi + o);
    };
    if (col) {
      if (SLEEP_W) __builtin_amdgcn_s_sleep(SLEEP_W);
      const int r = (t & 127) >> 3, k = t & 7, row = xc + 8 * (13 * cidx + r);
      const u32 o = k < 6 ? row * (DF * 2) + 128 * k : ((S - row) & (S - 1)) * (DF * 2) + (xc > 4 ? 640 : 0);
      warm((u64)(w < 2 ? fwr : fwi), S * DF * 2, X_NOW < 2 && r < 13 && k < 7 && row < S ? o & ~3u : NONE, ldsd);
      if (PF_MOD == 1) warm_mod();
    }
    PIN();
    const float kapf = (float)((g == 0 && c >= 12) ? c - 12 : c);
    const float k1f = g ? (float)g : (c >= 12 ? 4.f : 0.f);
    const int kind = g ? 0 : (c < 11 ? 1 : (c >= 12 && c < 23 ? 2 : 3));
    u32 selr = kind == 0 ? 0x03020100u : (kind == 1 ? 0x0c0c0100u : (kind == 2 ? 0x01000c0cu : 0x0c0c0c0cu));
    u32 seli = kind == 0 ? 0x01000302u : (kind == 1 ? 0x0c0c0302u : (kind == 2 ? 0x03020c0cu : 0x0c0c0c0cu));
    u32 selr2 = h ? 0x0c0c0c0cu : selr, seli2 = h ? 0x0c0c0c0cu : seli;
    keep(selr); keep(seli); keep(selr2); keep(seli2);
    u32x4 br[3], bi[3];
    f32x2 tc[8], ts[8];
    {
      const float h4 = (float)(4 * h), f1 = kapf * 0.05f, f2 = kapf * (-1.f / 640.f), f3 = (float)c * (1.f / 32.f);
      const float a1 = h4 * f1, a2 = h4 * f2 + (float)s2 * k1f * (-1.f / 2048.f), a3 = h4 * f3;
      Ph p1[2] = {phasor(a1, a1 + f1), phasor(a1 + 2.f * f1, a1 + 3.f * f1)}, p2[2] = {phasor(a2, a2 + f2), phasor(a2 + 2.f * f2, a2 + 3.f * f2)};
      Ph p3[2] = {phasor(a3, a3 + f3), phasor(a3 + 2.f * f3, a3 + 3.f * f3)};
      const Ph s1 = phasor(8.f * f1), s2_ = phasor(8.f * f2), s3 = phasor(8.f * f3);
#pragma unroll
      for (int tau = 0; tau < 4; ++tau)
#pragma unroll
        for (int e = 0; e < 2; ++e) {
          if (tau) { p2[e] = rot(p2[e], s2_); p3[e] = rot(p3[e], s3); }
          tc[2 * tau + e] = p2[e].c; ts[2 * tau + e] = p2[e].s;
          const f32x2 n = -p3[e].s;
#pragma unroll
          for (int k = 0; k < 2; ++k) { ar[tau][2 * e + k] = pk(p3[e].c[k], p3[e].s[k]); ai[tau][2 * e + k] = pk(n[k], p3[e].c[k]); }
          if (tau < 3) {
            if (tau) p1[e] = rot(p1[e], s1);
#pragma unroll
            for (int k = 0; k < 2; ++k) {
              const u32 p = pk(p1[e].c[k], p1[e].s[k]);
              br[tau][2 * e + k] = __builtin_amdgcn_perm(0u, p, tau == 2 ? selr2 : selr);
              bi[tau][2 * e + k] = __builtin_amdgcn_perm(0u, p ^ 0x80000000u, tau == 2 ? seli2 : seli);
            }
          }
        }
    }
#pragma unroll
    for (int i = 0; i < 3; ++i) { keep(br[i]); keep(bi[i]); }
#pragma unroll
    for (int i = 0; i < 4; ++i) { keep(ar[i]); keep(ai[i]); }
#pragma unroll
    for (int i = 0; i < 8; ++i) { keep(tc[i]); keep(ts[i]); }
    auto clear_slots = [&]() {
      const u32x4 sent = {SENT, SENT, SENT, SENT};
      const u32 o = O_T + (t >> 2) * SEGB + pos * 64 + (t & 3) * 16;
      st16(rs, o, sent);
      st16(rs, o + 64 * SEGB, sent);
      const u32 oh = O_H2 + s2 * 1280 + (t - 160) * 16, oc = (t < 248 ? O_CAND + (t - 240) * CAND_REP : O_DONE) + s2 * 16;
      st16(rs, t < 160 ? o + 128 * SEGB : (t < 240 ? oh : oc), sent);
      st16(rs, t < 32 && gu ? O_ACT + (t >> 2) * ACT_REP + gui * 64 + (t & 3) * 16 : NONE, sent);
    };
    PIN();
    if (CLR_POS == 0) clear_slots();
    asm volatile("" : "+s"(tag)::"memory");
    tlo = (u32)(tag + 1); thi = (u32)((tag + 1) >> 32);
    pub_tag = (l & 2) ? thi : tlo;
    keep(pub_off); keep(pub_sel); keep(pub_tag); keep(pub_src);
    STAMP(1);
    if (hasmod) {
      PRIO(3);
      BAR();
      if (w == 0) publish_mod(0, mb * 16);
      PRIO(0);
    }
    STAMP(3);
    BAR();
    if (CLR_POS == 1) clear_slots();
    DRAIN();
    if (__builtin_amdgcn_readfirstlane(l == 0 ? __hip_atomic_fetch_add(L + L_Q + 1, 1u, __ATOMIC_RELAXED, __HIP_MEMORY_SCOPE_WORKGROUP) : 0u) == 3u)
      st8(rs, l < 8 ? O_TOK + l * TOK_REP + b * 8 : NONE, (u32x2){tlo, thi});
    STAMP(2);
    if (PF_MOD == 3 && col) warm_mod();
    if (col) {
#pragma unroll
      for (int i = 0; i < WFIRST; ++i) ldw(i);
    }
    PIN();
    const u32* u = L + L_U + g * 640 + ((c >> 2) + 8 * (c & 3)) * 20;
    STAMP(6);
    PRIO(2);
    BAR();
    STAMP(4);
    f32x16 yr = {}, yi = {};
    u32x4 a[3];
#pragma unroll
    for (int tau = 0; tau < 3; ++tau) a[tau] = *(const u32x4*)(u + (tau == 2 ? 16 : 8 * tau + 4 * h));
#pragma unroll
    for (int tau = 0; tau < 3; ++tau) { yr = mfma32(a[tau], br[tau], yr); yi = mfma32(a[tau], bi[tau], yi); }
    f32x16 zr = {}, zi = {};
#pragma unroll
    for (int tau = 0; tau < 4; ++tau) {
      u32 o0, o1, o2, o3;
      cmul2(pr(yr, 4 * tau), pr(yi, 4 * tau), tc[2 * tau], ts[2 * tau], o0, o1);
      cmul2(pr(yr, 4 * tau + 2), pr(yi, 4 * tau + 2), tc[2 * tau + 1], ts[2 * tau + 1], o2, o3);
      const u32x4 v = {o0, o1, o2, o3};
      zr = mfma32(ar[tau], v, zr);
      zi = mfma32(ai[tau], v, zi);
      PIN();
    }
#pragma unroll
    for (int i = 0; i < 4; ++i) {
      old[i] = (u32x4){pk(zr[4 * i], zi[4 * i]), pk(zr[4 * i + 1], zi[4 * i + 1]), pk(zr[4 * i + 2], zi[4 * i + 2]), pk(zr[4 * i + 3], zi[4 * i + 3])};
      st16(rs, slot + i * sstep, old[i]);
      PIN();
    }
    PRIO(0);
    STAMP(5);
    u32 hr, hi;
    if (col) {
#pragma unroll
      for (int i = WFIRST; i < 4; ++i) ldw(i);
      const int ks = k1 + 8 * t;
      const u32 o2 = !X_NOW && herm ? ((mir ? ks : (S - ks) & (S - 1)) * DF + 20 * lam0) * 2 : NONE;
      hr = (u32)__builtin_amdgcn_raw_buffer_load_b16(__builtin_amdgcn_make_buffer_rsrc((void*)fwr, 0, S * DF * 2, 0x00020000), o2, 0, 0);
      hi = (u32)__builtin_amdgcn_raw_buffer_load_b16(__builtin_amdgcn_make_buffer_rsrc((void*)fwi, 0, S * DF * 2, 0x00020000), o2, 0, 0);
    }
    PIN();
    if (col) asm volatile("s_waitcnt vmcnt(%0)" ::"i"(2 * (4 - WFIRST) + 2 + B3_EARLY) : "memory");
    if (B3_SLEEP && col) __builtin_amdgcn_s_sleep(B3_SLEEP);
    BAR();
    if (w < 2 && hasmod) publish_mod(w * 256, 1280 + mb * 32 + w * 16);
    if (col) {
#pragma unroll
      for (int i = 0; i < 4; ++i) {
        u32x4 v = {__builtin_amdgcn_perm(wc[i][0], wa[i][0], 0x05040100), __builtin_amdgcn_perm(wc[i][0], wa[i][0], 0x07060302),
                   __builtin_amdgcn_perm(wc[i][1], wa[i][1], 0x05040100), __builtin_amdgcn_perm(wc[i][1], wa[i][1], 0x07060302)};
        if (mir) v = (u32x4){v[3] ^ 0x80000000u, v[2] ^ 0x80000000u, v[1] ^ 0x80000000u, v[0] ^ 0x80000000u};
        *(u32x4*)(L + L_W + (t + 256 * i) * 4) = v;
      }
      keep(hr); keep(hi);
      L[L_W2 + t] = __builtin_amdgcn_perm(hi, hr, 0x05040100) ^ (mir ? 0u : 0x80000000u);
    }
    __builtin_amdgcn_s_waitcnt(0x0F70);
    if (col) {
      BAR();
      if (PF_GU == 3) warm_gu();
      if (PF_DN == 3) warm_dn();
    }
    if (!act)
#pragma unroll
      for (int i = 0; i < 4; ++i) old[i] = (u32x4){SENT, SENT, SENT, SENT};
#pragma unroll
    for (int i = 0; i < 4; ++i) keep(old[i]);
    if (col) BAR(); else __builtin_amdgcn_s_sleep(SLEEP_M2);
    STAMP(7);
    {
      const u32 mo = O_MOD + xc * MOD_REP + 5120 + w4 * 2560 + l * 16, mo2 = l < 32 ? mo + 2048 : NONE;
      u32x4 g0 = ld16(rs, mo), g1 = ld16(rs, mo + 1024), g2 = ld16(rs, mo2);
      if (col) {
        if (PF_GU == 4) warm_gu();
        if (PF_DN == 4) warm_dn();
        BAR();
      }
    {
      const float f1 = (float)c * -0.05f, f2 = (float)c * (1.f / 640.f), h4 = (float)(4 * h), tw = (float)s2 * (1.f / 2048.f);
      float d1 = g == 0 && h ? -4.f : 8.f, d2 = g == 0 && !h ? -4.f : 8.f, e1 = g == 0 && h ? 4.f * tw : 0.f, e2 = g == 0 && !h ? 4.f * tw : 0.f;
      keep(d1); keep(d2); keep(e1); keep(e2);
      const float a1 = h4 * f1, a2 = h4 * f2 + (float)g * tw;
      Ph p1[2] = {phasor(a1, a1 + f1), phasor(a1 + 2.f * f1, a1 + 3.f * f1)}, p2[2] = {phasor(a2, a2 + f2), phasor(a2 + 2.f * f2, a2 + 3.f * f2)};
      const Ph s1[2] = {phasor(d1 * f1), phasor(d2 * f1)}, s2_[2] = {phasor(d1 * f2 + e1), phasor(d2 * f2 + e2)};
#pragma unroll
      for (int tau = 0; tau < 3; ++tau) {
        const bool up = g == 0 && (tau == 2 || (tau == 1 && h));
        u32 mr = up ? 0u : ~0u, si = g ? 0x05040706u : (up ? 0x03020100u : 0x0c0c0c0cu);
        keep(mr); keep(si);
#pragma unroll
        for (int e = 0; e < 2; ++e) {
          if (tau) { p1[e] = rot(p1[e], s1[tau - 1]); p2[e] = rot(p2[e], s2_[tau - 1]); }
          uc[2 * tau + e] = p2[e].c; us[2 * tau + e] = p2[e].s;
#pragma unroll
          for (int k = 0; k < 2; ++k) {
            const u32 p = pk(p1[e].c[k], p1[e].s[k]);
            cr[tau][2 * e + k] = p & mr;
            ci[tau][2 * e + k] = __builtin_amdgcn_perm(p ^ 0x80000000u, p, si);
          }
        }
      }
    }
#pragma unroll
    for (int i = 0; i < 3; ++i) { keep(cr[i]); keep(ci[i]); }
#pragma unroll
    for (int i = 0; i < 6; ++i) { keep(uc[i]); keep(us[i]); }
      auto miss = [&]() { return (g0[1] ^ tlo) | (g0[3] ^ thi) | (g1[1] ^ tlo) | (g1[3] ^ thi) | (l < 32 ? (g2[1] ^ tlo) | (g2[3] ^ thi) : 0u); };
      if (UNLIKELY(__any(miss() != 0))) {
        RETRY(0x10000);
        POLL(0u)
          __builtin_amdgcn_s_sleep(4);
          g0 = ld16(rs, mo); g1 = ld16(rs, mo + 1024); g2 = ld16(rs, mo2);
        UNTIL(miss(), 0x10000)
      }
      u32* lm = L + L_M + w4 * 320 + 2 * l;
      *(u32x2*)lm = (u32x2){g0[0], g0[2]}; *(u32x2*)(lm + 128) = (u32x2){g1[0], g1[2]};
      if (l < 32) *(u32x2*)(lm + 256) = (u32x2){g2[0], g2[2]};
    }
    DRAIN();
    if (!col) {
      const u32 co = l == 0 ? slot + 3 * sstep : NONE, want = l == 0 ? old[3][0] : SENT;
      POLL(SENT - 1)
        __builtin_amdgcn_s_sleep(2);
      UNTIL(~(ld4(rs, co) ^ want), 0)
    }
    BAR();
  }
  {
    STAMP(8);
    PRIO(2);
    u32x4 a[4];
#pragma unroll
    for (int i = 0; i < 4; ++i) a[i] = ld16(rs, slot + i * sstep);
    PIN();
    f32x16 yr = {}, yi = {};
    u32 top = 0;
#pragma unroll
    for (int i = 0; i < 4; ++i) {
      a[i] = ~(a[i] ^ old[i]);
      yr = mfma32(a[i], ai[i], yr); yi = mfma32(a[i], ar[i], yi);
      top = max(max(top, a[i][0]), a[i][1]); top = max(max(top, a[i][2]), a[i][3]);
      PIN();
    }
    if (UNLIKELY(__any(top == SENT))) {
      RETRY(1);
      POLL(SENT - 1)
#pragma unroll
        for (int i = 0; i < 4; ++i) a[i] = ~(ld16(rs, slot + i * sstep) ^ old[i]);
      UNTIL(max(max(umax4(a[0]), umax4(a[1])), max(umax4(a[2]), umax4(a[3]))), 1)
      yr = (f32x16){}; yi = (f32x16){};
#pragma unroll
      for (int i = 0; i < 4; ++i) { yr = mfma32(a[i], ai[i], yr); yi = mfma32(a[i], ar[i], yi); }
    }
    STAMP(9);
    f32x16 zr = {}, zi = {};
#pragma unroll
    for (int tau = 0; tau < 3; ++tau) {
      u32 o0, o1, o2, o3;
      cmul2(pr(yr, 4 * tau), pr(yi, 4 * tau), uc[2 * tau], us[2 * tau], o0, o1);
      cmul2(pr(yr, 4 * tau + 2), pr(yi, 4 * tau + 2), uc[2 * tau + 1], us[2 * tau + 1], o2, o3);
      const u32x4 v = {o0, o1, o2, o3};
      zr = mfma32(cr[tau], v, zr);
      zi = mfma32(ci[tau], v, zi);
      PIN();
    }
    u32* e = L + L_U + g * 640 + h * 128 + c;
#pragma unroll
    for (int r = 0; r < 8; ++r) e[(r >> 2) * 256 + (r & 3) * 32] = pk(zr[r], zi[r]);
    if (h == 0)
#pragma unroll
      for (int r = 8; r < 12; ++r) e[512 + (r & 3) * 32] = pk(zr[r], zi[r]);
    STAMP(10);
    BAR();
    PRIO(0);
  }
  u32 cs = l < 8 ? O_CAND + l * CAND_REP + s2 * 16 : NONE;
  keep(cs);
  PRIO(3);
  BAR();
  STAMP(11);
  if (w == 0) st4(rs, cs, L[L_Q]);
  if (col) { STAMP_OUT(); return; }
  auto best_key = [&]() {
    const u32 co = O_CAND + xc * CAND_REP + l * 16;
    u32 v = umax4((u32x4){ld4(rs, co), ld4(rs, co + 1024), ld4(rs, co + 2048), ld4(rs, co + 3072)});
    v = max(v, dppu<0xB1>(v)); v = max(v, dppu<0x4E>(v)); v = max(v, dppu<0x141>(v)); v = max(v, dppu<0x140>(v));
    const auto a = __builtin_amdgcn_permlane16_swap(v, v, false, false);
    v = max((u32)a[0], (u32)a[1]);
    const auto e = __builtin_amdgcn_permlane32_swap(v, v, false, false);
    return max((u32)e[0], (u32)e[1]);
  };
  if (gu) {
    u32x4 wf[20];
    const LAS u32* p = (const LAS u32*)(L + L_T) + (m16 & 2) * 5120 + gam * 128 + (m16 >> 2) * 16 + ((g ^ (2 * (gam & 1)) ^ ((m16 >> 1) & 1)) << 2) + (m16 & 1) * 2;
#pragma unroll
    for (int i = 0; i < 20; ++i) wf[i] = trfrag(p + i * 512, p + i * 512 + 64);
#pragma unroll
    for (int i = 0; i < 20; i += 4) asm volatile("" : "+v"(wf[i]), "+v"(wf[i + 1]), "+v"(wf[i + 2]), "+v"(wf[i + 3]));
    u32 ao = m16 < 8 ? O_ACT + (gam + 4 * (l & 1)) * ACT_REP + gui * 64 + g * 16 + (m16 >> 1) * 4 : NONE, ps = (l & 1) ? 0x01000504u : 0x05040100u;
    const LAS u32* hp = (const LAS u32*)(L + L_H) + (m16 & 3) * 16 + gam * 4;
    keep(ao); keep(ps); keep(hp);
    if (w == 0) {
      u32 hb = O_H2 + l * 16, hm = l < 16 ? 0u : NONE;
      keep(hb); keep(hm);
      DRAIN();
      if (SLEEP_A) __builtin_amdgcn_s_sleep(SLEEP_A);
      u32 k;
      POLL(SENT - 1)
        k = best_key();
      UNTIL(k, 0x100)
      STAMP(12);
      const u32 ho = hb + (k & 255) * 1280;
      POLL(SENT - 1)
        const u32x4 a = ld16(rs, ho), e = ld16(rs, (ho + 1024) | hm);
        *(u32x4*)(L + L_H + l * 4) = a;
        *(u32x4*)(L + L_H + 256 + l * 4) = e;
      UNTIL(max(umax4(a), umax4(e)), 0x10000)
    }
    BAR();
    STAMP(13);
    u32x4 hf[5];
#pragma unroll
    for (int i = 0; i < 5; ++i) hf[i] = *(const LAS u32x4*)(hp + i * 64);
    PIN();
    f32x4 d[4] = {z4, z4, z4, z4};
#pragma unroll
    for (int i = 0; i < 20; ++i) d[i & 3] = mfma16(hf[i >> 2], wf[i], d[i & 3]);
    const float dv = (d[0][0] + d[1][1]) + (d[2][2] + d[3][3]);
    const u32 gu2 = pk(dv, dppf<0x128>(dv));
    const float gg = lo16(gu2);
    const u32 av = pk(bfr(gg * sigm(gg)) * hi16(gu2), 0.f);
    st4(rs, ao, __builtin_amdgcn_perm(dppu<0xB1>(av), av, ps));
  } else {
    u32x4 wf[12];
    const LAS u32* p = (const LAS u32*)(L + L_T) + g * 3072 + gam * 64 + m16 * 2;
#pragma unroll
    for (int i = 0; i < 12; ++i) wf[i] = trfrag(p + (gam & 1) * 32 + i * 256, p + (~gam & 1) * 32 + i * 256);
#pragma unroll
    for (int i = 0; i < 12; i += 4) asm volatile("" : "+v"(wf[i]), "+v"(wf[i + 1]), "+v"(wf[i + 2]), "+v"(wf[i + 3]));
    const rsrc_t ro = __builtin_amdgcn_make_buffer_rsrc(out, 0, S * D * 2, 0x00020000);
    f32x2 base = {0, 0}, gs = {0, 0};
    u32 oo = NONE, k = 0;
    if (w == 0) {
      DRAIN();
      if (SLEEP_A) __builtin_amdgcn_s_sleep(SLEEP_A);
      POLL(SENT - 1)
        k = best_key();
      UNTIL(k, 0x100)
      STAMP(12);
    }
    BAR();
    if (w == 0) {
      oo = l < 8 ? (k & 2047) * 1280 + dni * 32 + l * 4 : NONE;
      POLL(SENT - 1)
      UNTIL(ld4(rs, O_DONE + (k & 255) * 16), 0)
      REREAD();
      base = up2(ld4(ro, oo));
      const u32 rb = (k & 0x80000000u) ? (k & 0x7FFFFFFFu) : ~k;
      gs = up2(L[L_M + 960 + dni * 8 + (l & 7)]) * bfr(sigm(bfr(asf(rb))));
      keep(base); keep(gs);
      if (SLEEP_C0) __builtin_amdgcn_s_sleep(SLEEP_C0);
    } else {
      __builtin_amdgcn_s_sleep(SLEEP_C);
    }
    const u32 ao = O_ACT + xc * ACT_REP + (12 * g + (m16 & 3)) * 64 + gam * 16;
    f32x4 d[4];
    POLL(SENT - 1)
      const u32x4 a0 = ld16(rs, ao), a1 = ld16(rs, ao + 256), a2 = ld16(rs, ao + 512);
      u32 top = max(umax4(a0), max(umax4(a1), umax4(a2)));
#pragma unroll
      for (int r = 0; r < 4; ++r) d[r] = mfma16(a0, wf[r], z4);
#pragma unroll
      for (int r = 0; r < 4; ++r) d[r] = mfma16(a1, wf[4 + r], d[r]);
      keep(top);
      PIN();
#pragma unroll
      for (int r = 0; r < 4; ++r) d[r] = mfma16(a2, wf[8 + r], d[r]);
    UNTIL(top, 0x10000)
    STAMP(13);
    Lf[L_Q + 16 + g * 64 + l] = (d[0][0] + d[1][1]) + (d[2][2] + d[3][3]);
    BAR();
    if (w == 0) {
      const float* q = Lf + L_Q + 16 + (l & 7) * 2;
      const f32x2 y = (*(const f32x2*)q + *(const f32x2*)(q + 64)) + (*(const f32x2*)(q + 128) + *(const f32x2*)(q + 192));
      const f32x2 o = base + gs * y;
      st4(ro, oo, pk(o[0], o[1]));
    }
  }
  STAMP(14);
  STAMP_OUT();
}

extern "C" void kernel_launch(void* const* d_in, const int* in_sizes, int n_in, void* d_out, int out_size, void* d_ws, size_t ws_size,
                              hipStream_t stream) {
  fused<<<256, 512, 0, stream>>>((const u16*)d_in[0], (const u16*)d_in[8], (const u16*)d_in[9], (char*)d_ws, (const u16*)d_in[1], (const u16*)d_in[2],
                                 (const u16*)d_in[4], (const u16*)d_in[5], (const u16*)d_in[6], (const u16*)d_in[7], (u16*)d_out);
}
```
